```python
import torch

def _softmax(x, axis=-1):
    return torch.softmax(x, dim=axis)
def _gelu(x):
    return torch.nn.functional.gelu(x, approximate="tanh")
def _silu(x):
    return torch.nn.functional.silu(x)
def _sigmoid(x):
    return torch.sigmoid(x)
def _layernorm(x, axis=-1, eps=1e-5):
    m = x.mean(dim=axis, keepdim=True)
    v = x.var(dim=axis, keepdim=True, unbiased=False)
    return (x - m) / torch.sqrt(v + eps)
def _scatter_add(tgt, idx, src, axis=0):
    return tgt.clone().index_add_(axis, idx, src)
def _segment_sum(src, seg, n):
    out = torch.zeros((n,) + src.shape[1:], dtype=src.dtype)
    return out.index_add_(0, seg, src)
def _pad(x, pw):
    flat = tuple(v for pair in reversed(pw) for v in pair)
    return torch.nn.functional.pad(x, flat)

kernel_name = 'interleave_layers__linear_kernel_hedgehog__spectral_fnet__prompt'

def setup_inputs():

    _w = (lambda a: (lambda t: t.to(torch.bfloat16) if t.is_floating_point() else t)(torch.as_tensor(a)))
    def _A_setup():
        B, S, D, H = 1, 2048, 576, 32
        Dh = D // H
        return {
            "x": _w((lambda *s: torch.randn(*s, dtype=torch.bfloat16) * 0.4)(B, S, D)),
            "wqkv": _w((lambda *s: torch.randn(*s, dtype=torch.bfloat16) * 1.6)(D, 3 * D)),
            "w_phi": _w((lambda *s: torch.randn(*s, dtype=torch.bfloat16) * 1.6)(H, Dh, Dh) * (Dh ** -0.5)),
            "wo": _w((lambda *s: torch.randn(*s, dtype=torch.bfloat16) * 1.6)(D, D)),
        }
    _A_in = _A_setup()

    def _B_setup():
        B, S, D = 1, 2048, 576
        ns = torch.arange(S)[:, None] * torch.arange(S)[None, :]
        nd = torch.arange(D)[:, None] * torch.arange(D)[None, :]
        Ws_re = torch.cos(-2.0 * torch.pi * ns / S).to(float)
        Ws_im = torch.sin(-2.0 * torch.pi * ns / S).to(float)
        Wd_re = torch.cos(-2.0 * torch.pi * nd / D).to(float)
        Wd_im = torch.sin(-2.0 * torch.pi * nd / D).to(float)
        return {
            "x": _w((lambda *s: torch.randn(*s, dtype=torch.bfloat16) * 0.4)(B, S, D)),
            "dft_seq_re": _w(Ws_re),
            "dft_seq_im": _w(Ws_im),
            "dft_hid_re": _w(Wd_re),
            "dft_hid_im": _w(Wd_im),
        }
    _B_in = _B_setup()

    return {
        "A_x": _A_in["x"],
        "A_wqkv": _A_in["wqkv"],
        "A_w_phi": _A_in["w_phi"],
        "A_wo": _A_in["wo"],
        "B_x": _B_in["x"],
        "B_dft_seq_re": _B_in["dft_seq_re"],
        "B_dft_seq_im": _B_in["dft_seq_im"],
        "B_dft_hid_re": _B_in["dft_hid_re"],
        "B_dft_hid_im": _B_in["dft_hid_im"],
    }

def reference(A_x, A_wqkv, A_w_phi, A_wo, B_x, B_dft_seq_re, B_dft_seq_im, B_dft_hid_re, B_dft_hid_im):
    def _A_ref(x, wqkv, w_phi, wo):
        B, S, D = x.shape
        H = 32; Dh = D // H
        qkv = (lambda x, *d: x.permute(*d))((x @ wqkv).reshape(B, S, 3, H, Dh), 2, 0, 3, 1, 4)
        q, k, v = qkv[0], qkv[1], qkv[2]
        pq = torch.einsum("bhsd,hdf->bhsf", q, w_phi)
        pk = torch.einsum("bhsd,hdf->bhsf", k, w_phi)
        phi_q = torch.concatenate([_softmax(pq, axis=-1), _softmax(-pq, axis=-1)], axis=-1)
        phi_k = torch.concatenate([_softmax(pk, axis=-1), _softmax(-pk, axis=-1)], axis=-1)
        kv = torch.einsum("bhsf,bhsd->bhfd", phi_k, v)
        z  = torch.einsum("bhsf->bhf", phi_k)
        num = torch.einsum("bhqf,bhfd->bhqd", phi_q, kv)
        den = torch.einsum("bhqf,bhf->bhq", phi_q, z)[..., None] + 1e-6
        o = num / den
        return (lambda x, *d: x.permute(*d))(o, 0, 2, 1, 3).reshape(B, S, D) @ wo

    def _B_ref(x, dft_seq_re, dft_seq_im, dft_hid_re, dft_hid_im):
        a = torch.einsum("bsd,ed->bse", x, dft_hid_re)
        b = torch.einsum("bsd,ed->bse", x, dft_hid_im)
        y = torch.einsum("ts,bse->bte", dft_seq_re, a) - torch.einsum("ts,bse->bte", dft_seq_im, b)
        return y

    x = A_x
    x = x + _A_ref(_layernorm(x, axis=-1), A_wqkv, A_w_phi, A_wo)
    x = x + _B_ref(_layernorm(x, axis=-1), B_dft_seq_re, B_dft_seq_im, B_dft_hid_re, B_dft_hid_im)
    x = x + _A_ref(_layernorm(x, axis=-1), A_wqkv, A_w_phi, A_wo)
    x = x + _B_ref(_layernorm(x, axis=-1), B_dft_seq_re, B_dft_seq_im, B_dft_hid_re, B_dft_hid_im)
    return x
```

```cpp
#include <hip/hip_runtime.h>
#include <hip/hip_bf16.h>
#include <stdint.h>

typedef unsigned short u16;
typedef __attribute__((ext_vector_type(8))) short bf16x8;
typedef __attribute__((ext_vector_type(4))) float f32x4;

#define S_LEN 2048
#define D_DIM 576
#define NH 32
#define DH 18
#define NF 36

__device__ __forceinline__ float bf2f(u16 v) { return __uint_as_float(((unsigned)v) << 16); }
__device__ __forceinline__ u16 f2bf(float f) {
  __hip_bfloat16 b = __float2bfloat16(f);
  return *reinterpret_cast<u16*>(&b);
}
__device__ __forceinline__ float rbf(float f) { return bf2f(f2bf(f)); }

__global__ void ln_kernel(const u16* __restrict__ x, u16* __restrict__ y) {
  int row = blockIdx.x * 4 + (threadIdx.x >> 6);
  int lane = threadIdx.x & 63;
  const u16* xr = x + row * D_DIM;
  float v[9];
  float s = 0.f;
  for (int i = 0; i < 9; i++) { v[i] = bf2f(xr[lane + 64 * i]); s += v[i]; }
  for (int o = 32; o; o >>= 1) s += __shfl_xor(s, o);
  float mean = s * (1.0f / D_DIM);
  float ss = 0.f;
  for (int i = 0; i < 9; i++) { float d = v[i] - mean; ss += d * d; }
  for (int o = 32; o; o >>= 1) ss += __shfl_xor(ss, o);
  float var = ss * (1.0f / D_DIM);
  float mb = rbf(mean), vb = rbf(var);
  float den = rbf(sqrtf(rbf(vb + 1e-5f)));
  for (int i = 0; i < 9; i++) {
    float d = rbf(v[i] - mb);
    y[row * D_DIM + lane + 64 * i] = f2bf(d / den);
  }
}

template <bool BKN>
__global__ __launch_bounds__(256) void gemm_kernel(const u16* __restrict__ A, int lda, const u16* __restrict__ B, int ldb,
                                                   u16* __restrict__ C, int ldc, const u16* __restrict__ res, int M, int N, int K,
                                                   int mode, float alpha) {
  __shared__ u16 sA[64][40];
  __shared__ u16 sB[64][40];
  int bm = blockIdx.y * 64, bn = blockIdx.x * 64;
  int tid = threadIdx.x, wid = tid >> 6, lane = tid & 63;
  int wr = wid >> 1, wc = wid & 1;
  f32x4 acc[2][2] = {};
  for (int k0 = 0; k0 < K; k0 += 32) {
    __syncthreads();
    for (int e = tid; e < 64 * 32; e += 256) {
      int r = e >> 5, c = e & 31;
      sA[r][c] = A[(long)(bm + r) * lda + k0 + c];
      if (BKN) {
        int kk = e >> 6, nn = e & 63;
        sB[nn][kk] = B[(long)(k0 + kk) * ldb + bn + nn];
      } else {
        sB[r][c] = B[(long)(bn + r) * ldb + k0 + c];
      }
    }
    __syncthreads();
    bf16x8 a[2], b[2];
    for (int i = 0; i < 2; i++) {
      for (int j = 0; j < 8; j++) {
        a[i][j] = sA[wr * 32 + i * 16 + (lane & 15)][(lane >> 4) * 8 + j];
        b[i][j] = sB[wc * 32 + i * 16 + (lane & 15)][(lane >> 4) * 8 + j];
      }
    }
    for (int i = 0; i < 2; i++)
      for (int j = 0; j < 2; j++) acc[i][j] = __builtin_amdgcn_mfma_f32_16x16x32_bf16(a[i], b[j], acc[i][j], 0, 0, 0);
  }
  for (int i = 0; i < 2; i++)
    for (int j = 0; j < 2; j++)
      for (int r = 0; r < 4; r++) {
        int m = bm + wr * 32 + i * 16 + (lane >> 4) * 4 + r;
        int n = bn + wc * 32 + j * 16 + (lane & 15);
        float v = acc[i][j][r] * alpha;
        if (mode == 0) C[(long)m * ldc + n] = f2bf(v);
        else if (mode == 1) C[(long)n * ldc + m] = f2bf(v);
        else C[(long)m * ldc + n] = f2bf(bf2f(res[(long)m * ldc + n]) + rbf(v));
      }
}

__global__ __launch_bounds__(256) void kv_kernel(const u16* __restrict__ qkv, const u16* __restrict__ wphi, float* __restrict__ kvout) {
  int h = blockIdx.x;
  __shared__ float sw[DH][DH];
  __shared__ float sphi[256][NF + 1];
  __shared__ float sv[256][19];
  int tid = threadIdx.x;
  for (int e = tid; e < DH * DH; e += 256) sw[e / DH][e % DH] = bf2f(wphi[h * DH * DH + e]);
  __syncthreads();
  float acc0 = 0.f, acc1 = 0.f, acc2 = 0.f;
  for (int s0 = 0; s0 < S_LEN; s0 += 256) {
    int s = s0 + tid;
    float k[DH], pk[DH];
#pragma unroll
    for (int d = 0; d < DH; d++) {
      k[d] = bf2f(qkv[(long)s * 1728 + 576 + h * DH + d]);
      sv[tid][d] = bf2f(qkv[(long)s * 1728 + 1152 + h * DH + d]);
    }
    sv[tid][18] = 1.f;
#pragma unroll
    for (int f = 0; f < DH; f++) {
      float a = 0.f;
#pragma unroll
      for (int d = 0; d < DH; d++) a += k[d] * sw[d][f];
      pk[f] = rbf(a);
    }
    float mx = -1e30f, mn = 1e30f;
#pragma unroll
    for (int f = 0; f < DH; f++) { mx = fmaxf(mx, pk[f]); mn = fminf(mn, pk[f]); }
    float e1[DH], e2[DH], s1 = 0.f, s2 = 0.f;
#pragma unroll
    for (int f = 0; f < DH; f++) { e1[f] = __expf(pk[f] - mx); s1 += e1[f]; e2[f] = __expf(mn - pk[f]); s2 += e2[f]; }
#pragma unroll
    for (int f = 0; f < DH; f++) { sphi[tid][f] = rbf(e1[f] / s1); sphi[tid][DH + f] = rbf(e2[f] / s2); }
    __syncthreads();
    for (int r = 0; r < 256; r++) {
      acc0 += sphi[r][tid / 19] * sv[r][tid % 19];
      acc1 += sphi[r][(tid + 256) / 19] * sv[r][(tid + 256) % 19];
      if (tid + 512 < NF * 19) acc2 += sphi[r][(tid + 512) / 19] * sv[r][(tid + 512) % 19];
    }
    __syncthreads();
  }
  kvout[h * NF * 19 + tid] = acc0;
  kvout[h * NF * 19 + tid + 256] = acc1;
  if (tid + 512 < NF * 19) kvout[h * NF * 19 + tid + 512] = acc2;
}

__global__ __launch_bounds__(256) void o_kernel(const u16* __restrict__ qkv, const u16* __restrict__ wphi, const float* __restrict__ kv,
                                                u16* __restrict__ o) {
  int idx = blockIdx.x * 256 + threadIdx.x;
  int s = idx / NH, h = idx % NH;
  float q[DH], pq[DH];
  for (int d = 0; d < DH; d++) q[d] = bf2f(qkv[(long)s * 1728 + h * DH + d]);
  for (int f = 0; f < DH; f++) {
    float a = 0.f;
    for (int d = 0; d < DH; d++) a += q[d] * bf2f(wphi[h * DH * DH + d * DH + f]);
    pq[f] = rbf(a);
  }
  float mx = -1e30f, mn = 1e30f;
  for (int f = 0; f < DH; f++) { mx = fmaxf(mx, pq[f]); mn = fminf(mn, pq[f]); }
  float e1[DH], e2[DH], s1 = 0.f, s2 = 0.f;
  for (int f = 0; f < DH; f++) { e1[f] = __expf(pq[f] - mx); s1 += e1[f]; e2[f] = __expf(mn - pq[f]); s2 += e2[f]; }
  float phi[NF];
  for (int f = 0; f < DH; f++) { phi[f] = rbf(e1[f] / s1); phi[DH + f] = rbf(e2[f] / s2); }
  float den = 0.f;
  for (int f = 0; f < NF; f++) den += phi[f] * rbf(kv[(h * NF + f) * 19 + 18]);
  den = rbf(rbf(den) + 1e-6f);
  for (int d = 0; d < DH; d++) {
    float num = 0.f;
    for (int f = 0; f < NF; f++) num += phi[f] * rbf(kv[(h * NF + f) * 19 + d]);
    o[(long)s * D_DIM + h * DH + d] = f2bf(rbf(num) / den);
  }
}

extern "C" void kernel_launch(void* const* d_in, const int* in_sizes, int n_in, void* d_out, int out_size, void* d_ws, size_t ws_size,
                              hipStream_t stream) {
  const u16* A_x = (const u16*)d_in[0];
  const u16* wqkv = (const u16*)d_in[1];
  const u16* wphi = (const u16*)d_in[2];
  const u16* wo = (const u16*)d_in[3];
  const u16* ws_re = (const u16*)d_in[5];
  const u16* ws_im = (const u16*)d_in[6];
  const u16* wd_re = (const u16*)d_in[7];
  const u16* wd_im = (const u16*)d_in[8];
  u16* out = (u16*)d_out;

  char* w = (char*)d_ws;
  u16* ln = (u16*)w; w += 2048 * 576 * 2;
  u16* qkv = (u16*)w; w += 2048 * 1728 * 2;
  u16* o = (u16*)w; w += 2048 * 576 * 2;
  u16* aT = (u16*)w; w += 2048 * 576 * 2;
  u16* bT = (u16*)w; w += 2048 * 576 * 2;
  u16* y1 = (u16*)w; w += 2048 * 576 * 2;
  u16* xa = (u16*)w; w += 2048 * 576 * 2;
  u16* xb = (u16*)w; w += 2048 * 576 * 2;
  float* kv = (float*)w; w += 32 * 36 * 19 * 4;

  const u16* x = A_x;
  for (int layer = 0; layer < 2; layer++) {
    ln_kernel<<<512, 256, 0, stream>>>(x, ln);
    gemm_kernel<true><<<dim3(1728 / 64, 2048 / 64), 256, 0, stream>>>(ln, 576, wqkv, 1728, qkv, 1728, nullptr, 2048, 1728, 576, 0, 1.f);
    kv_kernel<<<32, 256, 0, stream>>>(qkv, wphi, kv);
    o_kernel<<<2048 * 32 / 256, 256, 0, stream>>>(qkv, wphi, kv, o);
    gemm_kernel<true><<<dim3(576 / 64, 2048 / 64), 256, 0, stream>>>(o, 576, wo, 576, xa, 576, x, 2048, 576, 576, 2, 1.f);
    ln_kernel<<<512, 256, 0, stream>>>(xa, ln);
    gemm_kernel<false><<<dim3(576 / 64, 2048 / 64), 256, 0, stream>>>(ln, 576, wd_re, 576, aT, 2048, nullptr, 2048, 576, 576, 1, 1.f);
    gemm_kernel<false><<<dim3(576 / 64, 2048 / 64), 256, 0, stream>>>(ln, 576, wd_im, 576, bT, 2048, nullptr, 2048, 576, 576, 1, 1.f);
    gemm_kernel<false><<<dim3(576 / 64, 2048 / 64), 256, 0, stream>>>(ws_re, 2048, aT, 2048, y1, 576, nullptr, 2048, 576, 2048, 0, 1.f);
    gemm_kernel<false><<<dim3(576 / 64, 2048 / 64), 256, 0, stream>>>(ws_im, 2048, bT, 2048, xb, 576, y1, 2048, 576, 2048, 2, -1.f);
    u16* xo = (layer == 1) ? out : y1;
    {
      struct Dummy {};
    }
    extern __global__ void add_kernel(const u16*, const u16*, u16*);
    add_kernel<<<2048 * 576 / 256, 256, 0, stream>>>(xa, xb, xo);
    x = xo;
  }
}

__global__ void add_kernel(const u16* a, const u16* b, u16* c) {
  int i = blockIdx.x * 256 + threadIdx.x;
  c[i] = f2bf(bf2f(a[i]) + bf2f(b[i]));
}
```

```cpp
#include <hip/hip_runtime.h>
#include <hip/hip_bf16.h>
#include <stdint.h>

typedef unsigned short u16;
typedef __attribute__((ext_vector_type(8))) short bf16x8;
typedef __attribute__((ext_vector_type(4))) short s16x4;
typedef __attribute__((ext_vector_type(4))) float f32x4;
typedef __attribute__((ext_vector_type(4))) unsigned u32x4;
typedef __attribute__((ext_vector_type(2))) unsigned u32x2;
typedef __attribute__((address_space(3))) s16x4* lds_s16x4_ptr;

#define S_LEN 2048
#define D_DIM 576
#define NH 32
#define DH 18
#define NF 36
#define MFMA16 __builtin_amdgcn_mfma_f32_16x16x32_bf16

__device__ __forceinline__ float bf2f(u16 v) { return __uint_as_float(((unsigned)v) << 16); }
__device__ __forceinline__ u16 f2bf(float f) {
  __hip_bfloat16 b = __float2bfloat16(f);
  return *reinterpret_cast<u16*>(&b);
}
__device__ __forceinline__ float rbf(float f) { return bf2f(f2bf(f)); }
__device__ __forceinline__ unsigned pack2(float lo, float hi) { return (unsigned)f2bf(lo) | ((unsigned)f2bf(hi) << 16); }
__device__ __forceinline__ float lo2f(unsigned u) { return __uint_as_float(u << 16); }
__device__ __forceinline__ float hi2f(unsigned u) { return __uint_as_float(u & 0xffff0000u); }

__device__ __forceinline__ void glds16(const void* g, void* l) {
  __builtin_amdgcn_global_load_lds((const unsigned*)g, (unsigned*)l, 16, 0, 0);
}
__device__ __forceinline__ s16x4 tr_read(const char* p) {
  return __builtin_amdgcn_ds_read_tr16_b64_v4i16((lds_s16x4_ptr)(p));
}
#define WAIT_VM0() asm volatile("s_waitcnt vmcnt(0)" ::: "memory")

template <int NROWS>
__device__ __forceinline__ void ln_to_lds(const u16* __restrict__ x, char* lds, int wid, int lane) {
  const int sub = lane & 7, rr = lane >> 3;
  constexpr int RPW = NROWS / 4;
#pragma unroll
  for (int pass = 0; pass < RPW / 8; pass++) {
    const int row = wid * RPW + pass * 8 + rr;
    const u32x4* src = (const u32x4*)(x + (size_t)row * D_DIM);
    u32x4 v[9];
#pragma unroll
    for (int i = 0; i < 9; i++) v[i] = src[sub + 8 * i];
    float s = 0.f;
#pragma unroll
    for (int i = 0; i < 9; i++)
#pragma unroll
      for (int j = 0; j < 4; j++) s += lo2f(v[i][j]) + hi2f(v[i][j]);
    s += __shfl_xor(s, 1); s += __shfl_xor(s, 2); s += __shfl_xor(s, 4);
    const float mean = s * (1.0f / D_DIM);
    float ss = 0.f;
#pragma unroll
    for (int i = 0; i < 9; i++)
#pragma unroll
      for (int j = 0; j < 4; j++) {
        float a = lo2f(v[i][j]) - mean, b = hi2f(v[i][j]) - mean;
        ss += a * a + b * b;
      }
    ss += __shfl_xor(ss, 1); ss += __shfl_xor(ss, 2); ss += __shfl_xor(ss, 4);
    const float var = ss * (1.0f / D_DIM);
    const float mb = rbf(mean);
    const float den = rbf(sqrtf(rbf(rbf(var) + 1e-5f)));
#pragma unroll
    for (int i = 0; i < 9; i++) {
      u32x4 o;
#pragma unroll
      for (int j = 0; j < 4; j++) {
        float a = rbf(lo2f(v[i][j]) - mb) / den, b = rbf(hi2f(v[i][j]) - mb) / den;
        o[j] = pack2(a, b);
      }
      const int c = sub + 8 * i;
      *(u32x4*)(lds + row * 1152 + 16 * (c ^ (row & 7))) = o;
    }
  }
}
__device__ __forceinline__ bf16x8 lda_frag1152(const char* lds, int row, int chunk) {
  return *(const bf16x8*)(lds + row * 1152 + 16 * (chunk ^ (row & 7)));
}
__device__ __forceinline__ bf16x8 ld_frag128(const char* lds, int row, int chunk) {
  return *(const bf16x8*)(lds + row * 128 + 16 * (chunk ^ (row & 7)));
}

#define A1_BSTAGE 27648
__global__ __launch_bounds__(256) void k_a1(const u16* __restrict__ x, const u16* __restrict__ wqkv, const u16* __restrict__ wphi,
                                            u16* __restrict__ phiq, float* __restrict__ kvacc) {
  extern __shared__ __attribute__((aligned(16))) char smem[];
  char* sA = smem;
  char* sB = smem + 73728;
  const int tid = threadIdx.x, wid = tid >> 6, lane = tid & 63;
  const int bid = blockIdx.x;
  const int hg = bid & 7, mt = bid >> 3;
  const int wr = wid >> 1, wc = wid & 1;
  const int fr = lane & 15, fq = lane >> 4;

  auto stageB = [&](int buf, int kt) {
    char* dst = sB + buf * A1_BSTAGE;
#pragma unroll
    for (int j = 0; j < 7; j++) {
      int i = wid + 4 * j;
      if (i < 27) {
        int cid = i * 64 + lane;
        int krow = cid / 27, c = cid - krow * 27;
        int seg = c / 9, cc = c - seg * 9;
        const u16* g = wqkv + (size_t)(kt * 64 + krow) * 1728 + seg * 576 + hg * 72 + cc * 8;
        glds16(g, dst + i * 1024);
      }
    }
  };
  stageB(0, 0);
  ln_to_lds<64>(x + (size_t)mt * 64 * D_DIM, sA, wid, lane);

  f32x4 acc[2][7];
#pragma unroll
  for (int i = 0; i < 2; i++)
#pragma unroll
    for (int j = 0; j < 7; j++) acc[i][j] = (f32x4){0.f, 0.f, 0.f, 0.f};

  for (int kt = 0; kt < 9; kt++) {
    WAIT_VM0();
    __syncthreads();
    if (kt + 1 < 9) stageB((kt + 1) & 1, kt + 1);
    const char* bs = sB + (kt & 1) * A1_BSTAGE;
#pragma unroll
    for (int ks = 0; ks < 2; ks++) {
      bf16x8 a[2];
#pragma unroll
      for (int i = 0; i < 2; i++) a[i] = lda_frag1152(sA, wr * 32 + i * 16 + fr, kt * 8 + ks * 4 + fq);
#pragma unroll
      for (int j = 0; j < 7; j++) {
        const int t = wc * 7 + j;
        const int q = fr >> 2, p = fr & 3;
        const char* base = bs + (ks * 32 + 8 * fq + q) * 432 + (16 * t + 4 * p) * 2;
        s16x4 b0 = tr_read(base);
        s16x4 b1 = tr_read(base + 4 * 432);
        bf16x8 b = __builtin_shufflevector(b0, b1, 0, 1, 2, 3, 4, 5, 6, 7);
#pragma unroll
        for (int i = 0; i < 2; i++) acc[i][j] = MFMA16(a[i], b, acc[i][j], 0, 0, 0);
      }
    }
  }
  __syncthreads();
  u16* sQKV = (u16*)smem;
  u16* sP = (u16*)(smem + 49152);
  u16* sPhiT = (u16*)(smem + 69632);
  for (int e = tid; e < 3 * 4 * 64; e += 256) {
    u16* r = sQKV + e * 32;
    const bool isv = e >= 2 * 4 * 64;
    *(u32x4*)(r + 16) = (u32x4){0u, isv ? 0x3f80u : 0u, 0u, 0u};
    *(u32x4*)(r + 24) = (u32x4){0u, 0u, 0u, 0u};
  }
  __syncthreads();
#pragma unroll
  for (int j = 0; j < 7; j++) {
    const int col = (wc * 7 + j) * 16 + fr;
    if (col < 216) {
      const int sec = col / 72, cc = col - sec * 72;
      const int h = cc / 18, d = cc - h * 18;
#pragma unroll
      for (int i = 0; i < 2; i++)
#pragma unroll
        for (int r = 0; r < 4; r++) {
          const int s = wr * 32 + i * 16 + fq * 4 + r;
          sQKV[((sec * 4 + h) * 64 + s) * 32 + d] = f2bf(acc[i][j][r]);
        }
    }
  }
  __syncthreads();
  {
    const int h = wid;
    const u16* wp = wphi + (size_t)(hg * 4 + h) * DH * DH;
    bf16x8 bw[2];
#pragma unroll
    for (int ft = 0; ft < 2; ft++) {
      const int f = ft * 16 + fr;
#pragma unroll
      for (int j = 0; j < 8; j++) {
        const int d = fq * 8 + j;
        bw[ft][j] = (f < DH && d < DH) ? (short)wp[d * DH + f] : (short)0;
      }
    }
#pragma unroll
    for (int qk = 0; qk < 2; qk++)
#pragma unroll
      for (int rt = 0; rt < 4; rt++) {
        const int s = rt * 16 + fr;
        bf16x8 a = *(const bf16x8*)(sQKV + ((qk * 4 + h) * 64 + s) * 32 + fq * 8);
#pragma unroll
        for (int ft = 0; ft < 2; ft++) {
          f32x4 c = MFMA16(a, bw[ft], ((f32x4){0.f, 0.f, 0.f, 0.f}), 0, 0, 0);
          const int f = ft * 16 + fr;
          if (f < DH) {
#pragma unroll
            for (int r = 0; r < 4; r++) sP[((qk * 4 + h) * 64 + rt * 16 + fq * 4 + r) * 20 + f] = f2bf(c[r]);
          }
        }
      }
  }
  __syncthreads();
#pragma unroll
  for (int qk = 0; qk < 2; qk++) {
    const int h = tid >> 6, s = tid & 63;
    const u16* pp = sP + ((qk * 4 + h) * 64 + s) * 20;
    float pv[DH];
#pragma unroll
    for (int i = 0; i < 4; i++) {
      u32x2 w = *(const u32x2*)(pp + 4 * i);
      pv[4 * i] = lo2f(w[0]); pv[4 * i + 1] = hi2f(w[0]); pv[4 * i + 2] = lo2f(w[1]); pv[4 * i + 3] = hi2f(w[1]);
    }
    { unsigned w = *(const unsigned*)(pp + 16); pv[16] = lo2f(w); pv[17] = hi2f(w); }
    float mx = pv[0], mn = pv[0];
#pragma unroll
    for (int f = 1; f < DH; f++) { mx = fmaxf(mx, pv[f]); mn = fminf(mn, pv[f]); }
    float e1[DH], e2[DH], s1 = 0.f, s2 = 0.f;
#pragma unroll
    for (int f = 0; f < DH; f++) {
      e1[f] = __expf(pv[f] - mx); s1 += e1[f];
      e2[f] = __expf(mn - pv[f]); s2 += e2[f];
    }
    const float r1 = 1.0f / s1, r2 = 1.0f / s2;
    if (qk == 0) {
      unsigned o[20];
#pragma unroll
      for (int f = 0; f < 9; f++) o[f] = pack2(e1[2 * f] * r1, e1[2 * f + 1] * r1);
#pragma unroll
      for (int f = 0; f < 9; f++) o[9 + f] = pack2(e2[2 * f] * r2, e2[2 * f + 1] * r2);
      o[18] = 0u; o[19] = 0u;
      u32x4* dst = (u32x4*)(phiq + ((size_t)(hg * 4 + h) * S_LEN + mt * 64 + s) * 40);
#pragma unroll
      for (int i = 0; i < 5; i++) dst[i] = (u32x4){o[4 * i], o[4 * i + 1], o[4 * i + 2], o[4 * i + 3]};
    } else {
#pragma unroll
      for (int f = 0; f < DH; f++) {
        sPhiT[(h * 48 + f) * 64 + s] = f2bf(e1[f] * r1);
        sPhiT[(h * 48 + DH + f) * 64 + s] = f2bf(e2[f] * r2);
      }
    }
  }
  __syncthreads();
  {
    const int h = wid;
    const char* vb = (const char*)(sQKV + ((2 * 4 + h) * 64) * 32);
    f32x4 c[3][2];
#pragma unroll
    for (int ft = 0; ft < 3; ft++)
#pragma unroll
      for (int dt = 0; dt < 2; dt++) c[ft][dt] = (f32x4){0.f, 0.f, 0.f, 0.f};
#pragma unroll
    for (int ks = 0; ks < 2; ks++) {
      bf16x8 b[2];
      const int q = fr >> 2, p = fr & 3;
#pragma unroll
      for (int dt = 0; dt < 2; dt++) {
        const char* base = vb + (ks * 32 + 8 * fq + q) * 64 + (dt * 16 + 4 * p) * 2;
        s16x4 b0 = tr_read(base);
        s16x4 b1 = tr_read(base + 4 * 64);
        b[dt] = __builtin_shufflevector(b0, b1, 0, 1, 2, 3, 4, 5, 6, 7);
      }
#pragma unroll
      for (int ft = 0; ft < 3; ft++) {
        bf16x8 a = *(const bf16x8*)(sPhiT + (h * 48 + ft * 16 + fr) * 64 + ks * 32 + fq * 8);
#pragma unroll
        for (int dt = 0; dt < 2; dt++) c[ft][dt] = MFMA16(a, b[dt], c[ft][dt], 0, 0, 0);
      }
    }
    float* kvh = kvacc + (size_t)(hg * 4 + h) * NF * 20;
#pragma unroll
    for (int ft = 0; ft < 3; ft++)
#pragma unroll
      for (int dt = 0; dt < 2; dt++) {
        const int d = dt * 16 + fr;
#pragma unroll
        for (int r = 0; r < 4; r++) {
          const int f = ft * 16 + fq * 4 + r;
          if (f < NF && d < 19) atomicAdd(kvh + f * 20 + d, c[ft][dt][r]);
        }
      }
  }
}

#define A2_BSTAGE 18432
__global__ __launch_bounds__(256) void k_a2(const u16* __restrict__ phiq, const float* __restrict__ kvacc, const u16* __restrict__ wo,
                                            const u16* __restrict__ xin, u16* __restrict__ xout) {
  extern __shared__ __attribute__((aligned(16))) char smem[];
  char* sO = smem;
  u16* sKV = (u16*)(smem + 36864);
  char* sB = smem + 36864 + 51200;
  const int tid = threadIdx.x, wid = tid >> 6, lane = tid & 63;
  const int bid = blockIdx.x;
  const int nt = bid & 3, mt = bid >> 2;
  const int fr = lane & 15, fq = lane >> 4;

  auto stageB = [&](int buf, int kt) {
    char* dst = sB + buf * A2_BSTAGE;
#pragma unroll
    for (int j = 0; j < 5; j++) {
      int i = wid + 4 * j;
      if (i < 18) {
        int cid = i * 64 + lane;
        int krow = cid / 18, c = cid - krow * 18;
        const u16* g = wo + (size_t)(kt * 64 + krow) * D_DIM + nt * 144 + c * 8;
        glds16(g, dst + i * 1024);
      }
    }
  };
  stageB(0, 0);
  for (int e = tid; e < NH * 20 * 20; e += 256) {
    const int h = e / 400, rem = e - h * 400;
    const int d = rem / 20, fp = rem - d * 20;
    unsigned v = 0u;
    if (fp < 18 && d < 19) {
      const float* src = kvacc + (size_t)h * NF * 20 + d;
      v = pack2(src[(2 * fp) * 20], src[(2 * fp + 1) * 20]);
    }
    *(unsigned*)(sKV + (h * 20 + d) * 40 + 2 * fp) = v;
  }
  __syncthreads();
  for (int hh = 0; hh < 8; hh++) {
    const int h = wid * 8 + hh;
    bf16x8 b[2][2];
#pragma unroll
    for (int dt = 0; dt < 2; dt++) {
      int d = dt * 16 + fr;
      if (d > 19) d = 19;
      b[dt][0] = *(const bf16x8*)(sKV + (h * 20 + d) * 40 + fq * 8);
      bf16x8 z = {0, 0, 0, 0, 0, 0, 0, 0};
      b[dt][1] = (fq == 0) ? *(const bf16x8*)(sKV + (h * 20 + d) * 40 + 32) : z;
    }
#pragma unroll
    for (int rt = 0; rt < 2; rt++) {
      const u16* pq = phiq + ((size_t)h * S_LEN + mt * 32 + rt * 16 + fr) * 40;
      bf16x8 a0 = *(const bf16x8*)(pq + fq * 8);
      bf16x8 z = {0, 0, 0, 0, 0, 0, 0, 0};
      bf16x8 a1 = (fq == 0) ? *(const bf16x8*)(pq + 32) : z;
      f32x4 c0 = MFMA16(a0, b[0][0], ((f32x4){0.f, 0.f, 0.f, 0.f}), 0, 0, 0);
      c0 = MFMA16(a1, b[0][1], c0, 0, 0, 0);
      f32x4 c1 = MFMA16(a0, b[1][0], ((f32x4){0.f, 0.f, 0.f, 0.f}), 0, 0, 0);
      c1 = MFMA16(a1, b[1][1], c1, 0, 0, 0);
#pragma unroll
      for (int r = 0; r < 4; r++) {
        const float dn = __shfl(c1[r], (lane & 48) + 2);
        const float den = rbf(rbf(dn) + 1e-6f);
        const int s = rt * 16 + fq * 4 + r;
        const int col0 = h * DH + fr;
        *(u16*)(sO + s * 1152 + 16 * ((col0 >> 3) ^ (s & 7)) + (col0 & 7) * 2) = f2bf(rbf(c0[r]) / den);
        if (fr < 2) {
          const int col1 = h * DH + 16 + fr;
          *(u16*)(sO + s * 1152 + 16 * ((col1 >> 3) ^ (s & 7)) + (col1 & 7) * 2) = f2bf(rbf(c1[r]) / den);
        }
      }
    }
  }
  f32x4 acc[2][3];
#pragma unroll
  for (int i = 0; i < 2; i++)
#pragma unroll
    for (int j = 0; j < 3; j++) acc[i][j] = (f32x4){0.f, 0.f, 0.f, 0.f};
  const int ntl = (wid == 0) ? 3 : 2;
  for (int kt = 0; kt < 9; kt++) {
    WAIT_VM0();
    __syncthreads();
    if (kt + 1 < 9) stageB((kt + 1) & 1, kt + 1);
    const char* bs = sB + (kt & 1) * A2_BSTAGE;
#pragma unroll
    for (int ks = 0; ks < 2; ks++) {
      bf16x8 a[2];
#pragma unroll
      for (int i = 0; i < 2; i++) a[i] = lda_frag1152(sO, i * 16 + fr, kt * 8 + ks * 4 + fq);
#pragma unroll
      for (int j = 0; j < 3; j++) {
        if (j < ntl) {
          const int t = wid + 4 * j;
          const int q = fr >> 2, p = fr & 3;
          const char* base = bs + (ks * 32 + 8 * fq + q) * 288 + (16 * t + 4 * p) * 2;
          s16x4 b0 = tr_read(base);
          s16x4 b1 = tr_read(base + 4 * 288);
          bf16x8 b = __builtin_shufflevector(b0, b1, 0, 1, 2, 3, 4, 5, 6, 7);
#pragma unroll
          for (int i = 0; i < 2; i++) acc[i][j] = MFMA16(a[i], b, acc[i][j], 0, 0, 0);
        }
      }
    }
  }
#pragma unroll
  for (int j = 0; j < 3; j++) {
    if (j < ntl) {
      const int col = nt * 144 + (wid + 4 * j) * 16 + fr;
#pragma unroll
      for (int i = 0; i < 2; i++)
#pragma unroll
        for (int r = 0; r < 4; r++) {
          const size_t idx = (size_t)(mt * 32 + i * 16 + fq * 4 + r) * D_DIM + col;
          xout[idx] = f2bf(bf2f(xin[idx]) + rbf(acc[i][j][r]));
        }
    }
  }
}

#define B_BSTAGE 18432
__global__ __launch_bounds__(256) void k_b1(const u16* __restrict__ x, const u16* __restrict__ wdre, const u16* __restrict__ wdim,
                                            u16* __restrict__ aT, u16* __restrict__ bT) {
  extern __shared__ __attribute__((aligned(16))) char smem[];
  char* sA = smem;
  char* sB = smem + 73728;
  const int tid = threadIdx.x, wid = tid >> 6, lane = tid & 63;
  const int bid = blockIdx.x;
  const int nt = bid & 7, mt = bid >> 3;
  const int wr = wid >> 1, wc = wid & 1;
  const int fr = lane & 15, fq = lane >> 4;
  const u16* Bg = (nt < 4 ? wdre : wdim) + (size_t)((nt & 3) * 144) * D_DIM;
  auto stageB = [&](int buf, int kt) {
    char* dst = sB + buf * B_BSTAGE;
#pragma unroll
    for (int j = 0; j < 5; j++) {
      int i = wid + 4 * j;
      if (i < 18) {
        int row = i * 8 + (lane >> 3);
        int c = (lane & 7) ^ (row & 7);
        glds16(Bg + (size_t)row * D_DIM + kt * 64 + c * 8, dst + i * 1024);
      }
    }
  };
  stageB(0, 0);
  ln_to_lds<64>(x + (size_t)mt * 64 * D_DIM, sA, wid, lane);
  f32x4 acc[2][5];
#pragma unroll
  for (int i = 0; i < 2; i++)
#pragma unroll
    for (int j = 0; j < 5; j++) acc[i][j] = (f32x4){0.f, 0.f, 0.f, 0.f};
  const int ntl = wc ? 4 : 5;
  for (int kt = 0; kt < 9; kt++) {
    WAIT_VM0();
    __syncthreads();
    if (kt + 1 < 9) stageB((kt + 1) & 1, kt + 1);
    const char* bs = sB + (kt & 1) * B_BSTAGE;
#pragma unroll
    for (int ks = 0; ks < 2; ks++) {
      bf16x8 a[2];
#pragma unroll
      for (int i = 0; i < 2; i++) a[i] = lda_frag1152(sA, wr * 32 + i * 16 + fr, kt * 8 + ks * 4 + fq);
#pragma unroll
      for (int j = 0; j < 5; j++) {
        if (j < ntl) {
          bf16x8 b = ld_frag128(bs, (wc * 5 + j) * 16 + fr, ks * 4 + fq);
#pragma unroll
          for (int i = 0; i < 2; i++) acc[i][j] = MFMA16(a[i], b, acc[i][j], 0, 0, 0);
        }
      }
    }
  }
  u16* outp = (nt < 4 ? aT : bT);
#pragma unroll
  for (int j = 0; j < 5; j++) {
    if (j < ntl) {
      const int e = (nt & 3) * 144 + (wc * 5 + j) * 16 + fr;
#pragma unroll
      for (int i = 0; i < 2; i++) {
        const int s = mt * 64 + wr * 32 + i * 16 + fq * 4;
        u32x2 w = {pack2(acc[i][j][0], acc[i][j][1]), pack2(acc[i][j][2], acc[i][j][3])};
        *(u32x2*)(outp + (size_t)e * S_LEN + s) = w;
      }
    }
  }
}

__global__ __launch_bounds__(256) void k_b2(const u16* __restrict__ wre, const u16* __restrict__ wim, const u16* __restrict__ aT,
                                            const u16* __restrict__ bT, float* __restrict__ part) {
  extern __shared__ __attribute__((aligned(16))) char smem[];
  const int tid = threadIdx.x, wid = tid >> 6, lane = tid & 63;
  const int bid = blockIdx.x;
  const int xcd = bid & 7, idx = bid >> 3;
  const int kt4 = xcd >> 1, mt = (xcd & 1) * 8 + (idx >> 2), nt = idx & 3;
  const int wr = wid >> 1, wc = wid & 1;
  const int fr = lane & 15, fq = lane >> 4;
  const u16* Ag = (kt4 < 2 ? wre : wim) + (size_t)(mt * 128) * S_LEN + (kt4 & 1) * 1024;
  const u16* Bg = (kt4 < 2 ? aT : bT) + (size_t)(nt * 144) * S_LEN + (kt4 & 1) * 1024;
  auto stage = [&](int buf, int kt) {
    char* dA = smem + buf * 34816;
    char* dB = dA + 16384;
    const int rl = lane >> 3;
    const int c = (lane & 7) ^ (rl & 7);
#pragma unroll
    for (int j = 0; j < 4; j++) {
      int i = wid + 4 * j;
      glds16(Ag + (size_t)(i * 8 + rl) * S_LEN + kt * 64 + c * 8, dA + i * 1024);
    }
#pragma unroll
    for (int j = 0; j < 5; j++) {
      int i = wid + 4 * j;
      if (i < 18) glds16(Bg + (size_t)(i * 8 + rl) * S_LEN + kt * 64 + c * 8, dB + i * 1024);
    }
  };
  stage(0, 0);
  f32x4 acc[4][5];
#pragma unroll
  for (int i = 0; i < 4; i++)
#pragma unroll
    for (int j = 0; j < 5; j++) acc[i][j] = (f32x4){0.f, 0.f, 0.f, 0.f};
  const int ntl = wc ? 4 : 5;
  for (int kt = 0; kt < 16; kt++) {
    WAIT_VM0();
    __syncthreads();
    if (kt + 1 < 16) stage((kt + 1) & 1, kt + 1);
    const char* as = smem + (kt & 1) * 34816;
    const char* bs = as + 16384;
#pragma unroll
    for (int ks = 0; ks < 2; ks++) {
      bf16x8 a[4];
#pragma unroll
      for (int i = 0; i < 4; i++) a[i] = ld_frag128(as, wr * 64 + i * 16 + fr, ks * 4 + fq);
#pragma unroll
      for (int j = 0; j < 5; j++) {
        if (j < ntl) {
          bf16x8 b = ld_frag128(bs, (wc * 5 + j) * 16 + fr, ks * 4 + fq);
#pragma unroll
          for (int i = 0; i < 4; i++) acc[i][j] = MFMA16(a[i], b, acc[i][j], 0, 0, 0);
        }
      }
    }
  }
  float* pp = part + (size_t)kt4 * S_LEN * D_DIM;
#pragma unroll
  for (int j = 0; j < 5; j++) {
    if (j < ntl) {
      const int col = nt * 144 + (wc * 5 + j) * 16 + fr;
#pragma unroll
      for (int i = 0; i < 4; i++)
#pragma unroll
        for (int r = 0; r < 4; r++) pp[(size_t)(mt * 128 + wr * 64 + i * 16 + fq * 4 + r) * D_DIM + col] = acc[i][j][r];
    }
  }
}

__global__ __launch_bounds__(256) void k_red(const float* __restrict__ part, const u16* __restrict__ xin, u16* __restrict__ xout) {
  const int i = (blockIdx.x * 256 + threadIdx.x) * 4;
  const size_t N = (size_t)S_LEN * D_DIM;
  f32x4 p0 = *(const f32x4*)(part + i), p1 = *(const f32x4*)(part + N + i);
  f32x4 p2 = *(const f32x4*)(part + 2 * N + i), p3 = *(const f32x4*)(part + 3 * N + i);
  u32x2 xv = *(const u32x2*)(xin + i);
  float xr[4] = {lo2f(xv[0]), hi2f(xv[0]), lo2f(xv[1]), hi2f(xv[1])};
  float o[4];
#pragma unroll
  for (int j = 0; j < 4; j++) o[j] = xr[j] + rbf(rbf(p0[j] + p1[j]) - rbf(p2[j] + p3[j]));
  *(u32x2*)(xout + i) = (u32x2){pack2(o[0], o[1]), pack2(o[2], o[3])};
}

extern "C" void kernel_launch(void* const* d_in, const int* in_sizes, int n_in, void* d_out, int out_size, void* d_ws, size_t ws_size,
                              hipStream_t stream) {
  const u16* A_x = (const u16*)d_in[0];
  const u16* wqkv = (const u16*)d_in[1];
  const u16* wphi = (const u16*)d_in[2];
  const u16* wo = (const u16*)d_in[3];
  const u16* ws_re = (const u16*)d_in[5];
  const u16* ws_im = (const u16*)d_in[6];
  const u16* wd_re = (const u16*)d_in[7];
  const u16* wd_im = (const u16*)d_in[8];
  u16* out = (u16*)d_out;

  char* w = (char*)d_ws;
  float* kvacc = (float*)w; w += 2 * NH * NF * 20 * 4;
  u16* phiq = (u16*)w; w += (size_t)NH * S_LEN * 40 * 2;
  u16* x1 = (u16*)w; w += S_LEN * D_DIM * 2;
  u16* x2 = (u16*)w; w += S_LEN * D_DIM * 2;
  u16* aT = (u16*)w; w += S_LEN * D_DIM * 2;
  u16* bT = (u16*)w; w += S_LEN * D_DIM * 2;
  float* part = (float*)w; w += (size_t)4 * S_LEN * D_DIM * 4;

  {
    hipFuncSetAttribute((const void*)k_a1, hipFuncAttributeMaxDynamicSharedMemorySize, 73728 + 2 * A1_BSTAGE + 64);
    hipFuncSetAttribute((const void*)k_a2, hipFuncAttributeMaxDynamicSharedMemorySize, 36864 + 51200 + 2 * A2_BSTAGE + 64);
    hipFuncSetAttribute((const void*)k_b1, hipFuncAttributeMaxDynamicSharedMemorySize, 73728 + 2 * B_BSTAGE);
    hipFuncSetAttribute((const void*)k_b2, hipFuncAttributeMaxDynamicSharedMemorySize, 2 * 34816);
  }
  hipMemsetAsync(kvacc, 0, 2 * NH * NF * 20 * 4, stream);
  const u16* x = A_x;
  for (int layer = 0; layer < 2; layer++) {
    float* kva = kvacc + layer * NH * NF * 20;
    k_a1<<<256, 256, 73728 + 2 * A1_BSTAGE + 64, stream>>>(x, wqkv, wphi, phiq, kva);
    k_a2<<<256, 256, 36864 + 51200 + 2 * A2_BSTAGE + 64, stream>>>(phiq, kva, wo, x, x1);
    k_b1<<<256, 256, 73728 + 2 * B_BSTAGE, stream>>>(x1, wd_re, wd_im, aT, bT);
    k_b2<<<256, 256, 2 * 34816, stream>>>(ws_re, ws_im, aT, bT, part);
    u16* xo = layer == 0 ? x2 : out;
    k_red<<<S_LEN * D_DIM / 1024, 256, 0, stream>>>(part, x1, xo);
    x = xo;
  }
}
```

```cpp
#include <hip/hip_runtime.h>
#include <hip/hip_bf16.h>
#include <stdint.h>

typedef unsigned short u16;
typedef __attribute__((ext_vector_type(8))) short bf16x8;
typedef __attribute__((ext_vector_type(4))) short s16x4;
typedef __attribute__((ext_vector_type(4))) float f32x4;
typedef __attribute__((ext_vector_type(4))) unsigned u32x4;
typedef __attribute__((ext_vector_type(2))) unsigned u32x2;
typedef __attribute__((address_space(3))) s16x4* lds_s16x4_ptr;

#define S_LEN 2048
#define D_DIM 576
#define NH 32
#define DH 18
#define NF 36
#define MFMA16 __builtin_amdgcn_mfma_f32_16x16x32_bf16

__device__ __forceinline__ float bf2f(u16 v) { return __uint_as_float(((unsigned)v) << 16); }
__device__ __forceinline__ u16 f2bf(float f) {
  __hip_bfloat16 b = __float2bfloat16(f);
  return *reinterpret_cast<u16*>(&b);
}
__device__ __forceinline__ float rbf(float f) { return bf2f(f2bf(f)); }
__device__ __forceinline__ unsigned pack2(float lo, float hi) { return (unsigned)f2bf(lo) | ((unsigned)f2bf(hi) << 16); }
__device__ __forceinline__ float lo2f(unsigned u) { return __uint_as_float(u << 16); }
__device__ __forceinline__ float hi2f(unsigned u) { return __uint_as_float(u & 0xffff0000u); }

__device__ __forceinline__ void glds16(const void* g, void* l) {
  __builtin_amdgcn_global_load_lds((const unsigned*)g, (unsigned*)l, 16, 0, 0);
}
__device__ __forceinline__ s16x4 tr_read(const char* p) {
  return __builtin_amdgcn_ds_read_tr16_b64_v4i16((lds_s16x4_ptr)(p));
}
#define WAIT_VM0() asm volatile("s_waitcnt vmcnt(0)" ::: "memory")

__device__ __forceinline__ void ln64_to_lds(const u16* __restrict__ x, char* lds, int wid, int lane) {
  const int sub = lane & 7, rr = lane >> 3;
  const int row = wid * 8 + rr;
  const u32x4* src = (const u32x4*)(x + (size_t)row * D_DIM);
  u32x4 v[9];
#pragma unroll
  for (int i = 0; i < 9; i++) v[i] = src[sub + 8 * i];
  float s = 0.f;
#pragma unroll
  for (int i = 0; i < 9; i++)
#pragma unroll
    for (int j = 0; j < 4; j++) s += lo2f(v[i][j]) + hi2f(v[i][j]);
  s += __shfl_xor(s, 1); s += __shfl_xor(s, 2); s += __shfl_xor(s, 4);
  const float mean = s * (1.0f / D_DIM);
  float ss = 0.f;
#pragma unroll
  for (int i = 0; i < 9; i++)
#pragma unroll
    for (int j = 0; j < 4; j++) {
      float a = lo2f(v[i][j]) - mean, b = hi2f(v[i][j]) - mean;
      ss += a * a + b * b;
    }
  ss += __shfl_xor(ss, 1); ss += __shfl_xor(ss, 2); ss += __shfl_xor(ss, 4);
  const float var = ss * (1.0f / D_DIM);
  const float mb = rbf(mean);
  const float rden = 1.0f / rbf(sqrtf(rbf(rbf(var) + 1e-5f)));
#pragma unroll
  for (int i = 0; i < 9; i++) {
    u32x4 o;
#pragma unroll
    for (int j = 0; j < 4; j++) {
      float a = rbf(lo2f(v[i][j]) - mb) * rden, b = rbf(hi2f(v[i][j]) - mb) * rden;
      o[j] = pack2(a, b);
    }
    const int c = sub + 8 * i;
    *(u32x4*)(lds + row * 1152 + 16 * (c ^ (row & 7))) = o;
  }
}
__device__ __forceinline__ bf16x8 lda_frag1152(const char* lds, int row, int chunk) {
  return *(const bf16x8*)(lds + row * 1152 + 16 * (chunk ^ (row & 7)));
}
__device__ __forceinline__ bf16x8 ld_frag128(const char* lds, int row, int chunk) {
  return *(const bf16x8*)(lds + row * 128 + 16 * (chunk ^ (row & 7)));
}
#define WAITV(n) asm volatile("s_waitcnt vmcnt(" #n ")" ::: "memory")
#define BARRIER() do { asm volatile("s_waitcnt lgkmcnt(0)" ::: "memory"); __builtin_amdgcn_s_barrier(); } while (0)

#define A1_BSTAGE 27648
#define A1_NST 3
__global__ __launch_bounds__(512) void k_a1(const u16* __restrict__ x, const u16* __restrict__ wqkv, const u16* __restrict__ wphi,
                                            u16* __restrict__ phiq, float* __restrict__ kvacc) {
  extern __shared__ __attribute__((aligned(16))) char smem[];
  char* sA = smem;
  char* sB = smem + 73728;
  const int tid = threadIdx.x, wid = tid >> 6, lane = tid & 63;
  const int bid = blockIdx.x;
  const int hg = bid & 7, mt = bid >> 3;
  const int wr = wid >> 2, wc = wid & 3;
  const int fr = lane & 15, fq = lane >> 4;

  auto stageB = [&](int buf, int kt) {
    char* dst = sB + buf * A1_BSTAGE;
#pragma unroll
    for (int j = 0; j < 4; j++) {
      int i = wid + 8 * j;
      if (i < 27) {
        int cid = i * 64 + lane;
        int krow = cid / 27, c = cid - krow * 27;
        int seg = c / 9, cc = c - seg * 9;
        const u16* g = wqkv + (size_t)(kt * 64 + krow) * 1728 + seg * 576 + hg * 72 + cc * 8;
        glds16(g, dst + i * 1024);
      }
    }
  };
  stageB(0, 0);
  stageB(1, 1);
  ln64_to_lds(x + (size_t)mt * 64 * D_DIM, sA, wid, lane);

  f32x4 acc[2][4];
#pragma unroll
  for (int i = 0; i < 2; i++)
#pragma unroll
    for (int j = 0; j < 4; j++) acc[i][j] = (f32x4){0.f, 0.f, 0.f, 0.f};
  const int ntl = (wc == 3) ? 2 : 4;

  for (int kt = 0; kt < 9; kt++) {
    if (kt < 8) { if (wid < 3) WAITV(4); else WAITV(3); } else WAITV(0);
    BARRIER();
    if (kt + 2 < 9) stageB((kt + 2) % 3, kt + 2);
    const char* bs = sB + (kt % 3) * A1_BSTAGE;
#pragma unroll
    for (int ks = 0; ks < 2; ks++) {
      bf16x8 a[2];
#pragma unroll
      for (int i = 0; i < 2; i++) a[i] = lda_frag1152(sA, wr * 32 + i * 16 + fr, kt * 8 + ks * 4 + fq);
#pragma unroll
      for (int j = 0; j < 4; j++) {
        if (j < ntl) {
          const int t = wc * 4 + j;
          const int q = fr >> 2, p = fr & 3;
          const char* base = bs + (ks * 32 + 8 * fq + q) * 432 + (16 * t + 4 * p) * 2;
          s16x4 b0 = tr_read(base);
          s16x4 b1 = tr_read(base + 4 * 432);
          bf16x8 b = __builtin_shufflevector(b0, b1, 0, 1, 2, 3, 4, 5, 6, 7);
#pragma unroll
          for (int i = 0; i < 2; i++) acc[i][j] = MFMA16(a[i], b, acc[i][j], 0, 0, 0);
        }
      }
    }
  }
  __syncthreads();
  u16* sQKV = (u16*)smem;
  u16* sP = (u16*)(smem + 49152);
  u16* sPhiT = (u16*)(smem + 69632);
  for (int e = tid; e < 3 * 4 * 64; e += 512) {
    u16* r = sQKV + e * 32;
    const bool isv = e >= 2 * 4 * 64;
    *(u32x4*)(r + 16) = (u32x4){0u, isv ? 0x3f80u : 0u, 0u, 0u};
    *(u32x4*)(r + 24) = (u32x4){0u, 0u, 0u, 0u};
  }
  __syncthreads();
#pragma unroll
  for (int j = 0; j < 4; j++) {
    const int col = (wc * 4 + j) * 16 + fr;
    if (j < ntl && col < 216) {
      const int sec = col / 72, cc = col - sec * 72;
      const int h = cc / 18, d = cc - h * 18;
#pragma unroll
      for (int i = 0; i < 2; i++)
#pragma unroll
        for (int r = 0; r < 4; r++) {
          const int s = wr * 32 + i * 16 + fq * 4 + r;
          sQKV[((sec * 4 + h) * 64 + s) * 32 + d] = f2bf(acc[i][j][r]);
        }
    }
  }
  __syncthreads();
  {
    const int h = wid & 3, qk = wid >> 2;
    const u16* wp = wphi + (size_t)(hg * 4 + h) * DH * DH;
    bf16x8 bw[2];
#pragma unroll
    for (int ft = 0; ft < 2; ft++) {
      const int f = ft * 16 + fr;
#pragma unroll
      for (int j = 0; j < 8; j++) {
        const int d = fq * 8 + j;
        bw[ft][j] = (f < DH && d < DH) ? (short)wp[d * DH + f] : (short)0;
      }
    }
#pragma unroll
    for (int rt = 0; rt < 4; rt++) {
      const int s = rt * 16 + fr;
      bf16x8 a = *(const bf16x8*)(sQKV + ((qk * 4 + h) * 64 + s) * 32 + fq * 8);
#pragma unroll
      for (int ft = 0; ft < 2; ft++) {
        f32x4 c = MFMA16(a, bw[ft], ((f32x4){0.f, 0.f, 0.f, 0.f}), 0, 0, 0);
        const int f = ft * 16 + fr;
        if (f < DH) {
#pragma unroll
          for (int r = 0; r < 4; r++) sP[((qk * 4 + h) * 64 + rt * 16 + fq * 4 + r) * 20 + f] = f2bf(c[r]);
        }
      }
    }
  }
  __syncthreads();
  {
    const int qk = tid >> 8, h = (tid >> 6) & 3, s = tid & 63;
    const u16* pp = sP + ((qk * 4 + h) * 64 + s) * 20;
    float pv[DH];
#pragma unroll
    for (int i = 0; i < 4; i++) {
      u32x2 w = *(const u32x2*)(pp + 4 * i);
      pv[4 * i] = lo2f(w[0]); pv[4 * i + 1] = hi2f(w[0]); pv[4 * i + 2] = lo2f(w[1]); pv[4 * i + 3] = hi2f(w[1]);
    }
    { unsigned w = *(const unsigned*)(pp + 16); pv[16] = lo2f(w); pv[17] = hi2f(w); }
    float mx = pv[0], mn = pv[0];
#pragma unroll
    for (int f = 1; f < DH; f++) { mx = fmaxf(mx, pv[f]); mn = fminf(mn, pv[f]); }
    float e1[DH], e2[DH], s1 = 0.f, s2 = 0.f;
#pragma unroll
    for (int f = 0; f < DH; f++) {
      e1[f] = __expf(pv[f] - mx); s1 += e1[f];
      e2[f] = __expf(mn - pv[f]); s2 += e2[f];
    }
    const float r1 = 1.0f / s1, r2 = 1.0f / s2;
    if (qk == 0) {
      unsigned o[20];
#pragma unroll
      for (int f = 0; f < 9; f++) o[f] = pack2(e1[2 * f] * r1, e1[2 * f + 1] * r1);
#pragma unroll
      for (int f = 0; f < 9; f++) o[9 + f] = pack2(e2[2 * f] * r2, e2[2 * f + 1] * r2);
      o[18] = 0u; o[19] = 0u;
      u32x4* dst = (u32x4*)(phiq + ((size_t)(hg * 4 + h) * S_LEN + mt * 64 + s) * 40);
#pragma unroll
      for (int i = 0; i < 5; i++) dst[i] = (u32x4){o[4 * i], o[4 * i + 1], o[4 * i + 2], o[4 * i + 3]};
    } else {
#pragma unroll
      for (int f = 0; f < DH; f++) {
        sPhiT[(h * 48 + f) * 64 + s] = f2bf(e1[f] * r1);
        sPhiT[(h * 48 + DH + f) * 64 + s] = f2bf(e2[f] * r2);
      }
    }
  }
  __syncthreads();
  {
    const int h = wid & 3, ks = wid >> 2;
    const char* vb = (const char*)(sQKV + ((2 * 4 + h) * 64) * 32);
    f32x4 c[3][2];
    bf16x8 b[2];
    const int q = fr >> 2, p = fr & 3;
#pragma unroll
    for (int dt = 0; dt < 2; dt++) {
      const char* base = vb + (ks * 32 + 8 * fq + q) * 64 + (dt * 16 + 4 * p) * 2;
      s16x4 b0 = tr_read(base);
      s16x4 b1 = tr_read(base + 4 * 64);
      b[dt] = __builtin_shufflevector(b0, b1, 0, 1, 2, 3, 4, 5, 6, 7);
    }
#pragma unroll
    for (int ft = 0; ft < 3; ft++) {
      bf16x8 a = *(const bf16x8*)(sPhiT + (h * 48 + ft * 16 + fr) * 64 + ks * 32 + fq * 8);
#pragma unroll
      for (int dt = 0; dt < 2; dt++) c[ft][dt] = MFMA16(a, b[dt], ((f32x4){0.f, 0.f, 0.f, 0.f}), 0, 0, 0);
    }
    float* kvh = kvacc + (size_t)(hg * 4 + h) * NF * 20;
#pragma unroll
    for (int ft = 0; ft < 3; ft++)
#pragma unroll
      for (int dt = 0; dt < 2; dt++) {
        const int d = dt * 16 + fr;
#pragma unroll
        for (int r = 0; r < 4; r++) {
          const int f = ft * 16 + fq * 4 + r;
          if (f < NF && d < 19) atomicAdd(kvh + f * 20 + d, c[ft][dt][r]);
        }
      }
  }
}

#define A2_BSTAGE 18432
__global__ __launch_bounds__(512) void k_a2(const u16* __restrict__ phiq, const float* __restrict__ kvacc, const u16* __restrict__ wo,
                                            const u16* __restrict__ xin, u16* __restrict__ xout) {
  extern __shared__ __attribute__((aligned(16))) char smem[];
  char* sO = smem;
  u16* sKV = (u16*)(smem + 36864);
  char* sB = smem + 36864 + 51200;
  const int tid = threadIdx.x, wid = tid >> 6, lane = tid & 63;
  const int bid = blockIdx.x;
  const int nt = bid & 3, mt = bid >> 2;
  const int fr = lane & 15, fq = lane >> 4;

  auto stageB = [&](int buf, int kt) {
    char* dst = sB + buf * A2_BSTAGE;
#pragma unroll
    for (int j = 0; j < 3; j++) {
      int i = wid + 8 * j;
      if (i < 18) {
        int cid = i * 64 + lane;
        int krow = cid / 18, c = cid - krow * 18;
        const u16* g = wo + (size_t)(kt * 64 + krow) * D_DIM + nt * 144 + c * 8;
        glds16(g, dst + i * 1024);
      }
    }
  };
  stageB(0, 0);
  stageB(1, 1);
  bf16x8 pa[4][2][2];
#pragma unroll
  for (int hh = 0; hh < 4; hh++)
#pragma unroll
    for (int rt = 0; rt < 2; rt++) {
      const u16* pq = phiq + ((size_t)(wid * 4 + hh) * S_LEN + mt * 32 + rt * 16 + fr) * 40;
      pa[hh][rt][0] = *(const bf16x8*)(pq + fq * 8);
      pa[hh][rt][1] = *(const bf16x8*)(pq + 32);
    }
  for (int e = tid; e < NH * 20 * 4; e += 512) {
    const int hd = e >> 2;
    *(u32x2*)(sKV + hd * 40 + 32 + 0) = (u32x2){0u, 0u};
  }
  for (int e = tid; e < NH * NF * 5; e += 512) {
    const int hf = e / 5, dq = e - hf * 5;
    const int h = hf / NF, f = hf - h * NF;
    f32x4 v = *(const f32x4*)(kvacc + (size_t)hf * 20 + dq * 4);
#pragma unroll
    for (int j = 0; j < 4; j++) sKV[(h * 20 + dq * 4 + j) * 40 + f] = f2bf(v[j]);
  }
  __syncthreads();
#pragma unroll
  for (int hh = 0; hh < 4; hh++) {
    const int h = wid * 4 + hh;
    bf16x8 b[2][2];
    const bf16x8 z = {0, 0, 0, 0, 0, 0, 0, 0};
#pragma unroll
    for (int dt = 0; dt < 2; dt++) {
      int d = dt * 16 + fr;
      if (d > 19) d = 19;
      b[dt][0] = *(const bf16x8*)(sKV + (h * 20 + d) * 40 + fq * 8);
      b[dt][1] = (fq == 0) ? *(const bf16x8*)(sKV + (h * 20 + d) * 40 + 32) : z;
    }
#pragma unroll
    for (int rt = 0; rt < 2; rt++) {
      bf16x8 a0 = pa[hh][rt][0];
      bf16x8 a1 = (fq == 0) ? pa[hh][rt][1] : z;
      f32x4 c0 = MFMA16(a0, b[0][0], ((f32x4){0.f, 0.f, 0.f, 0.f}), 0, 0, 0);
      c0 = MFMA16(a1, b[0][1], c0, 0, 0, 0);
      f32x4 c1 = MFMA16(a0, b[1][0], ((f32x4){0.f, 0.f, 0.f, 0.f}), 0, 0, 0);
      c1 = MFMA16(a1, b[1][1], c1, 0, 0, 0);
#pragma unroll
      for (int r = 0; r < 4; r++) {
        const float dn = __shfl(c1[r], (lane & 48) + 2);
        const float den = rbf(rbf(dn) + 1e-6f);
        const int s = rt * 16 + fq * 4 + r;
        const int col0 = h * DH + fr;
        *(u16*)(sO + s * 1152 + 16 * ((col0 >> 3) ^ (s & 7)) + (col0 & 7) * 2) = f2bf(rbf(c0[r]) / den);
        if (fr < 2) {
          const int col1 = h * DH + 16 + fr;
          *(u16*)(sO + s * 1152 + 16 * ((col1 >> 3) ^ (s & 7)) + (col1 & 7) * 2) = f2bf(rbf(c1[r]) / den);
        }
      }
    }
  }
  f32x4 acc[3];
#pragma unroll
  for (int j = 0; j < 3; j++) acc[j] = (f32x4){0.f, 0.f, 0.f, 0.f};
  const int rt = wid & 1, ct = wid >> 1;
  const int ntl = (ct == 0) ? 3 : 2;
  for (int kt = 0; kt < 9; kt++) {
    if (kt < 8) { if (wid < 2) WAITV(3); else WAITV(2); } else WAITV(0);
    BARRIER();
    if (kt + 2 < 9) stageB((kt + 2) % 3, kt + 2);
    const char* bs = sB + (kt % 3) * A2_BSTAGE;
#pragma unroll
    for (int ks = 0; ks < 2; ks++) {
      bf16x8 a = lda_frag1152(sO, rt * 16 + fr, kt * 8 + ks * 4 + fq);
#pragma unroll
      for (int j = 0; j < 3; j++) {
        if (j < ntl) {
          const int t = ct + 4 * j;
          const int q = fr >> 2, p = fr & 3;
          const char* base = bs + (ks * 32 + 8 * fq + q) * 288 + (16 * t + 4 * p) * 2;
          s16x4 b0 = tr_read(base);
          s16x4 b1 = tr_read(base + 4 * 288);
          bf16x8 b = __builtin_shufflevector(b0, b1, 0, 1, 2, 3, 4, 5, 6, 7);
          acc[j] = MFMA16(a, b, acc[j], 0, 0, 0);
        }
      }
    }
  }
#pragma unroll
  for (int j = 0; j < 3; j++) {
    if (j < ntl) {
      const int col = nt * 144 + (ct + 4 * j) * 16 + fr;
#pragma unroll
      for (int r = 0; r < 4; r++) {
        const size_t idx = (size_t)(mt * 32 + rt * 16 + fq * 4 + r) * D_DIM + col;
        xout[idx] = f2bf(bf2f(xin[idx]) + rbf(acc[j][r]));
      }
    }
  }
}

#define B_BSTAGE 18432
__global__ __launch_bounds__(512) void k_b1(const u16* __restrict__ x, const u16* __restrict__ wdre, const u16* __restrict__ wdim,
                                            u16* __restrict__ aT, u16* __restrict__ bT) {
  extern __shared__ __attribute__((aligned(16))) char smem[];
  char* sA = smem;
  char* sB = smem + 73728;
  const int tid = threadIdx.x, wid = tid >> 6, lane = tid & 63;
  const int bid = blockIdx.x;
  const int nt = bid & 7, mt = bid >> 3;
  const int wr = wid >> 1, wc = wid & 1;
  const int fr = lane & 15, fq = lane >> 4;
  const u16* Bg = (nt < 4 ? wdre : wdim) + (size_t)((nt & 3) * 144) * D_DIM;
  auto stageB = [&](int buf, int kt) {
    char* dst = sB + buf * B_BSTAGE;
#pragma unroll
    for (int j = 0; j < 3; j++) {
      int i = wid + 8 * j;
      if (i < 18) {
        int row = i * 8 + (lane >> 3);
        int c = (lane & 7) ^ (row & 7);
        glds16(Bg + (size_t)row * D_DIM + kt * 64 + c * 8, dst + i * 1024);
      }
    }
  };
  stageB(0, 0);
  stageB(1, 1);
  stageB(2, 2);
  ln64_to_lds(x + (size_t)mt * 64 * D_DIM, sA, wid, lane);
  f32x4 acc[5];
#pragma unroll
  for (int j = 0; j < 5; j++) acc[j] = (f32x4){0.f, 0.f, 0.f, 0.f};
  const int ntl = wc ? 4 : 5;
  for (int kt = 0; kt < 9; kt++) {
    if (kt < 7) { if (wid < 2) WAITV(6); else WAITV(4); }
    else if (kt == 7) { if (wid < 2) WAITV(3); else WAITV(2); }
    else WAITV(0);
    BARRIER();
    if (kt + 3 < 9) stageB((kt + 3) & 3, kt + 3);
    const char* bs = sB + (kt & 3) * B_BSTAGE;
#pragma unroll
    for (int ks = 0; ks < 2; ks++) {
      bf16x8 a = lda_frag1152(sA, wr * 16 + fr, kt * 8 + ks * 4 + fq);
#pragma unroll
      for (int j = 0; j < 5; j++) {
        if (j < ntl) {
          bf16x8 b = ld_frag128(bs, (wc * 5 + j) * 16 + fr, ks * 4 + fq);
          acc[j] = MFMA16(a, b, acc[j], 0, 0, 0);
        }
      }
    }
  }
  u16* outp = (nt < 4 ? aT : bT);
#pragma unroll
  for (int j = 0; j < 5; j++) {
    if (j < ntl) {
      const int e = (nt & 3) * 144 + (wc * 5 + j) * 16 + fr;
      const int s = mt * 64 + wr * 16 + fq * 4;
      u32x2 w = {pack2(acc[j][0], acc[j][1]), pack2(acc[j][2], acc[j][3])};
      *(u32x2*)(outp + (size_t)e * S_LEN + s) = w;
    }
  }
}

__global__ __launch_bounds__(512) void k_b2(const u16* __restrict__ wre, const u16* __restrict__ wim, const u16* __restrict__ aT,
                                            const u16* __restrict__ bT, float* __restrict__ part) {
  extern __shared__ __attribute__((aligned(16))) char smem[];
  const int tid = threadIdx.x, wid = tid >> 6, lane = tid & 63;
  const int bid = blockIdx.x;
  const int xcd = bid & 7, idx = bid >> 3;
  const int kt4 = xcd >> 1, mt = (xcd & 1) * 8 + (idx >> 2), nt = idx & 3;
  const int wr = wid >> 1, wc = wid & 1;
  const int fr = lane & 15, fq = lane >> 4;
  const u16* Ag = (kt4 < 2 ? wre : wim) + (size_t)(mt * 128) * S_LEN + (kt4 & 1) * 1024;
  const u16* Bg = (kt4 < 2 ? aT : bT) + (size_t)(nt * 144) * S_LEN + (kt4 & 1) * 1024;
  auto stage = [&](int buf, int kt) {
    char* dA = smem + buf * 34816;
    char* dB = dA + 16384;
    const int rl = lane >> 3;
    const int c = (lane & 7) ^ (rl & 7);
#pragma unroll
    for (int j = 0; j < 2; j++) {
      int i = wid + 8 * j;
      glds16(Ag + (size_t)(i * 8 + rl) * S_LEN + kt * 64 + c * 8, dA + i * 1024);
    }
#pragma unroll
    for (int j = 0; j < 3; j++) {
      int i = wid + 8 * j;
      if (i < 18) glds16(Bg + (size_t)(i * 8 + rl) * S_LEN + kt * 64 + c * 8, dB + i * 1024);
    }
  };
  stage(0, 0);
  stage(1, 1);
  stage(2, 2);
  f32x4 acc[2][5];
#pragma unroll
  for (int i = 0; i < 2; i++)
#pragma unroll
    for (int j = 0; j < 5; j++) acc[i][j] = (f32x4){0.f, 0.f, 0.f, 0.f};
  const int ntl = wc ? 4 : 5;
  for (int kt = 0; kt < 16; kt++) {
    if (kt < 14) { if (wid < 2) WAITV(10); else WAITV(8); }
    else if (kt == 14) { if (wid < 2) WAITV(5); else WAITV(4); }
    else WAITV(0);
    BARRIER();
    if (kt + 3 < 16) stage((kt + 3) & 3, kt + 3);
    const char* as = smem + (kt & 3) * 34816;
    const char* bs = as + 16384;
    bf16x8 a[2][2], b[2][5];
#pragma unroll
    for (int ks = 0; ks < 2; ks++) {
#pragma unroll
      for (int i = 0; i < 2; i++) a[ks][i] = ld_frag128(as, wr * 32 + i * 16 + fr, ks * 4 + fq);
#pragma unroll
      for (int j = 0; j < 5; j++) b[ks][j] = ld_frag128(bs, ((wc * 5 + j) % 9) * 16 + fr, ks * 4 + fq);
    }
#pragma unroll
    for (int ks = 0; ks < 2; ks++)
#pragma unroll
      for (int j = 0; j < 5; j++) {
        if (j < ntl) {
#pragma unroll
          for (int i = 0; i < 2; i++) acc[i][j] = MFMA16(a[ks][i], b[ks][j], acc[i][j], 0, 0, 0);
        }
      }
  }
  float* pp = part + (size_t)kt4 * S_LEN * D_DIM;
#pragma unroll
  for (int j = 0; j < 5; j++) {
    if (j < ntl) {
      const int col = nt * 144 + (wc * 5 + j) * 16 + fr;
#pragma unroll
      for (int i = 0; i < 2; i++)
#pragma unroll
        for (int r = 0; r < 4; r++) pp[(size_t)(mt * 128 + wr * 32 + i * 16 + fq * 4 + r) * D_DIM + col] = acc[i][j][r];
    }
  }
}
__global__ __launch_bounds__(256) void k_red(const float* __restrict__ part, const u16* __restrict__ xin, u16* __restrict__ xout) {
  const int i = (blockIdx.x * 256 + threadIdx.x) * 4;
  const size_t N = (size_t)S_LEN * D_DIM;
  f32x4 p0 = *(const f32x4*)(part + i), p1 = *(const f32x4*)(part + N + i);
  f32x4 p2 = *(const f32x4*)(part + 2 * N + i), p3 = *(const f32x4*)(part + 3 * N + i);
  u32x2 xv = *(const u32x2*)(xin + i);
  float xr[4] = {lo2f(xv[0]), hi2f(xv[0]), lo2f(xv[1]), hi2f(xv[1])};
  float o[4];
#pragma unroll
  for (int j = 0; j < 4; j++) o[j] = xr[j] + rbf(rbf(p0[j] + p1[j]) - rbf(p2[j] + p3[j]));
  *(u32x2*)(xout + i) = (u32x2){pack2(o[0], o[1]), pack2(o[2], o[3])};
}

extern "C" void kernel_launch(void* const* d_in, const int* in_sizes, int n_in, void* d_out, int out_size, void* d_ws, size_t ws_size,
                              hipStream_t stream) {
  const u16* A_x = (const u16*)d_in[0];
  const u16* wqkv = (const u16*)d_in[1];
  const u16* wphi = (const u16*)d_in[2];
  const u16* wo = (const u16*)d_in[3];
  const u16* ws_re = (const u16*)d_in[5];
  const u16* ws_im = (const u16*)d_in[6];
  const u16* wd_re = (const u16*)d_in[7];
  const u16* wd_im = (const u16*)d_in[8];
  u16* out = (u16*)d_out;

  char* w = (char*)d_ws;
  float* kvacc = (float*)w; w += 2 * NH * NF * 20 * 4;
  u16* phiq = (u16*)w; w += (size_t)NH * S_LEN * 40 * 2;
  u16* x1 = (u16*)w; w += S_LEN * D_DIM * 2;
  u16* x2 = (u16*)w; w += S_LEN * D_DIM * 2;
  u16* aT = (u16*)w; w += S_LEN * D_DIM * 2;
  u16* bT = (u16*)w; w += S_LEN * D_DIM * 2;
  float* part = (float*)w; w += (size_t)4 * S_LEN * D_DIM * 4;

  {
    hipFuncSetAttribute((const void*)k_a1, hipFuncAttributeMaxDynamicSharedMemorySize, 73728 + 3 * A1_BSTAGE + 64);
    hipFuncSetAttribute((const void*)k_a2, hipFuncAttributeMaxDynamicSharedMemorySize, 36864 + 51200 + 3 * A2_BSTAGE + 64);
    hipFuncSetAttribute((const void*)k_b1, hipFuncAttributeMaxDynamicSharedMemorySize, 73728 + 4 * B_BSTAGE);
    hipFuncSetAttribute((const void*)k_b2, hipFuncAttributeMaxDynamicSharedMemorySize, 4 * 34816);
  }
  hipMemsetAsync(kvacc, 0, 2 * NH * NF * 20 * 4, stream);
  const u16* x = A_x;
  for (int layer = 0; layer < 2; layer++) {
    float* kva = kvacc + layer * NH * NF * 20;
    k_a1<<<256, 512, 73728 + 3 * A1_BSTAGE + 64, stream>>>(x, wqkv, wphi, phiq, kva);
    k_a2<<<256, 512, 36864 + 51200 + 3 * A2_BSTAGE + 64, stream>>>(phiq, kva, wo, x, x1);
    k_b1<<<256, 512, 73728 + 4 * B_BSTAGE, stream>>>(x1, wd_re, wd_im, aT, bT);
    k_b2<<<256, 512, 4 * 34816, stream>>>(ws_re, ws_im, aT, bT, part);
    u16* xo = layer == 0 ? x2 : out;
    k_red<<<S_LEN * D_DIM / 1024, 256, 0, stream>>>(part, x1, xo);
    x = xo;
  }
}
```

```cpp
#include <hip/hip_runtime.h>
#include <hip/hip_bf16.h>
#include <stdint.h>

typedef unsigned short u16;
typedef __attribute__((ext_vector_type(8))) short bf16x8;
typedef __attribute__((ext_vector_type(4))) short s16x4;
typedef __attribute__((ext_vector_type(4))) float f32x4;
typedef __attribute__((ext_vector_type(4))) unsigned u32x4;
typedef __attribute__((ext_vector_type(2))) unsigned u32x2;
typedef __attribute__((address_space(3))) s16x4* lds_s16x4_ptr;

#define S_LEN 2048
#define D_DIM 576
#define NH 32
#define DH 18
#define NF 36
#define MFMA16 __builtin_amdgcn_mfma_f32_16x16x32_bf16

__device__ __forceinline__ float bf2f(u16 v) { return __uint_as_float(((unsigned)v) << 16); }
__device__ __forceinline__ u16 f2bf(float f) {
  __hip_bfloat16 b = __float2bfloat16(f);
  return *reinterpret_cast<u16*>(&b);
}
__device__ __forceinline__ float rbf(float f) { return bf2f(f2bf(f)); }
__device__ __forceinline__ unsigned pack2(float lo, float hi) { return (unsigned)f2bf(lo) | ((unsigned)f2bf(hi) << 16); }
__device__ __forceinline__ float lo2f(unsigned u) { return __uint_as_float(u << 16); }
__device__ __forceinline__ float hi2f(unsigned u) { return __uint_as_float(u & 0xffff0000u); }

__device__ __forceinline__ void glds16(const void* g, void* l) {
  __builtin_amdgcn_global_load_lds((const unsigned*)g, (unsigned*)l, 16, 0, 0);
}
__device__ __forceinline__ s16x4 tr_read(const char* p) {
  return __builtin_amdgcn_ds_read_tr16_b64_v4i16((lds_s16x4_ptr)(p));
}
#define WAIT_VM0() asm volatile("s_waitcnt vmcnt(0)" ::: "memory")

__device__ __forceinline__ void ln64_to_lds(const u16* __restrict__ x, char* lds, int wid, int lane) {
  const int sub = lane & 7, rr = lane >> 3;
  const int row = wid * 8 + rr;
  const u32x4* src = (const u32x4*)(x + (size_t)row * D_DIM);
  u32x4 v[9];
#pragma unroll
  for (int i = 0; i < 9; i++) v[i] = src[sub + 8 * i];
  float s = 0.f;
#pragma unroll
  for (int i = 0; i < 9; i++)
#pragma unroll
    for (int j = 0; j < 4; j++) s += lo2f(v[i][j]) + hi2f(v[i][j]);
  s += __shfl_xor(s, 1); s += __shfl_xor(s, 2); s += __shfl_xor(s, 4);
  const float mean = s * (1.0f / D_DIM);
  float ss = 0.f;
#pragma unroll
  for (int i = 0; i < 9; i++)
#pragma unroll
    for (int j = 0; j < 4; j++) {
      float a = lo2f(v[i][j]) - mean, b = hi2f(v[i][j]) - mean;
      ss += a * a + b * b;
    }
  ss += __shfl_xor(ss, 1); ss += __shfl_xor(ss, 2); ss += __shfl_xor(ss, 4);
  const float var = ss * (1.0f / D_DIM);
  const float mb = rbf(mean);
  const float rden = 1.0f / rbf(sqrtf(rbf(rbf(var) + 1e-5f)));
#pragma unroll
  for (int i = 0; i < 9; i++) {
    u32x4 o;
#pragma unroll
    for (int j = 0; j < 4; j++) {
      float a = rbf(lo2f(v[i][j]) - mb) * rden, b = rbf(hi2f(v[i][j]) - mb) * rden;
      o[j] = pack2(a, b);
    }
    const int c = sub + 8 * i;
    *(u32x4*)(lds + row * 1152 + 16 * (c ^ (row & 7))) = o;
  }
}
__device__ __forceinline__ bf16x8 lda_frag1152(const char* lds, int row, int chunk) {
  return *(const bf16x8*)(lds + row * 1152 + 16 * (chunk ^ (row & 7)));
}
__device__ __forceinline__ bf16x8 ld_frag128(const char* lds, int row, int chunk) {
  return *(const bf16x8*)(lds + row * 128 + 16 * (chunk ^ (row & 7)));
}
#define WAITV(n) asm volatile("s_waitcnt vmcnt(" #n ")" ::: "memory")
#define BARRIER() do { asm volatile("s_waitcnt lgkmcnt(0)" ::: "memory"); __builtin_amdgcn_s_barrier(); } while (0)

#define A1_BSTAGE 27648
#define A1_NST 3
__global__ __launch_bounds__(512) void k_a1(const u16* __restrict__ x, const u16* __restrict__ wqkv, const u16* __restrict__ wphi,
                                            u16* __restrict__ phiq, float* __restrict__ kvacc) {
  extern __shared__ __attribute__((aligned(16))) char smem[];
  char* sA = smem;
  char* sB = smem + 73728;
  const int tid = threadIdx.x, wid = tid >> 6, lane = tid & 63;
  const int bid = blockIdx.x;
  const int hg = bid & 7, mt = bid >> 3;
  const int wr = wid >> 2, wc = wid & 3;
  const int fr = lane & 15, fq = lane >> 4;

  auto stageB = [&](int buf, int kt) {
    char* dst = sB + buf * A1_BSTAGE;
#pragma unroll
    for (int j = 0; j < 4; j++) {
      int i = wid + 8 * j;
      if (i < 27) {
        int cid = i * 64 + lane;
        int krow = cid / 27, c = cid - krow * 27;
        int seg = c / 9, cc = c - seg * 9;
        const u16* g = wqkv + (size_t)(kt * 64 + krow) * 1728 + seg * 576 + hg * 72 + cc * 8;
        glds16(g, dst + i * 1024);
      }
    }
  };
  stageB(0, 0);
  stageB(1, 1);
  ln64_to_lds(x + (size_t)mt * 64 * D_DIM, sA, wid, lane);

  f32x4 acc[2][4];
#pragma unroll
  for (int i = 0; i < 2; i++)
#pragma unroll
    for (int j = 0; j < 4; j++) acc[i][j] = (f32x4){0.f, 0.f, 0.f, 0.f};
  const int ntl = (wc == 3) ? 2 : 4;

  for (int kt = 0; kt < 9; kt++) {
    if (kt < 8) { if (wid < 3) WAITV(4); else WAITV(3); } else WAITV(0);
    BARRIER();
    if (kt + 2 < 9) stageB((kt + 2) % 3, kt + 2);
    const char* bs = sB + (kt % 3) * A1_BSTAGE;
#pragma unroll
    for (int ks = 0; ks < 2; ks++) {
      bf16x8 a[2];
#pragma unroll
      for (int i = 0; i < 2; i++) a[i] = lda_frag1152(sA, wr * 32 + i * 16 + fr, kt * 8 + ks * 4 + fq);
#pragma unroll
      for (int j = 0; j < 4; j++) {
        if (j < ntl) {
          const int t = wc * 4 + j;
          const int q = fr >> 2, p = fr & 3;
          const char* base = bs + (ks * 32 + 8 * fq + q) * 432 + (16 * t + 4 * p) * 2;
          s16x4 b0 = tr_read(base);
          s16x4 b1 = tr_read(base + 4 * 432);
          bf16x8 b = __builtin_shufflevector(b0, b1, 0, 1, 2, 3, 4, 5, 6, 7);
#pragma unroll
          for (int i = 0; i < 2; i++) acc[i][j] = MFMA16(a[i], b, acc[i][j], 0, 0, 0);
        }
      }
    }
  }
  __syncthreads();
  u16* sQKV = (u16*)smem;
  u16* sP = (u16*)(smem + 49152);
  u16* sPhiT = (u16*)(smem + 69632);
  for (int e = tid; e < 3 * 4 * 64; e += 512) {
    u16* r = sQKV + e * 32;
    const bool isv = e >= 2 * 4 * 64;
    *(u32x4*)(r + 16) = (u32x4){0u, isv ? 0x3f80u : 0u, 0u, 0u};
    *(u32x4*)(r + 24) = (u32x4){0u, 0u, 0u, 0u};
  }
  __syncthreads();
#pragma unroll
  for (int j = 0; j < 4; j++) {
    const int col = (wc * 4 + j) * 16 + fr;
    if (j < ntl && col < 216) {
      const int sec = col / 72, cc = col - sec * 72;
      const int h = cc / 18, d = cc - h * 18;
#pragma unroll
      for (int i = 0; i < 2; i++)
#pragma unroll
        for (int r = 0; r < 4; r++) {
          const int s = wr * 32 + i * 16 + fq * 4 + r;
          sQKV[((sec * 4 + h) * 64 + s) * 32 + d] = f2bf(acc[i][j][r]);
        }
    }
  }
  __syncthreads();
  {
    const int h = wid & 3, qk = wid >> 2;
    const u16* wp = wphi + (size_t)(hg * 4 + h) * DH * DH;
    bf16x8 bw[2];
#pragma unroll
    for (int ft = 0; ft < 2; ft++) {
      const int f = ft * 16 + fr;
#pragma unroll
      for (int j = 0; j < 8; j++) {
        const int d = fq * 8 + j;
        bw[ft][j] = (f < DH && d < DH) ? (short)wp[d * DH + f] : (short)0;
      }
    }
#pragma unroll
    for (int rt = 0; rt < 4; rt++) {
      const int s = rt * 16 + fr;
      bf16x8 a = *(const bf16x8*)(sQKV + ((qk * 4 + h) * 64 + s) * 32 + fq * 8);
#pragma unroll
      for (int ft = 0; ft < 2; ft++) {
        f32x4 c = MFMA16(a, bw[ft], ((f32x4){0.f, 0.f, 0.f, 0.f}), 0, 0, 0);
        const int f = ft * 16 + fr;
        if (f < DH) {
#pragma unroll
          for (int r = 0; r < 4; r++) sP[((qk * 4 + h) * 64 + rt * 16 + fq * 4 + r) * 20 + f] = f2bf(c[r]);
        }
      }
    }
  }
  __syncthreads();
  {
    const int qk = tid >> 8, h = (tid >> 6) & 3, s = tid & 63;
    const u16* pp = sP + ((qk * 4 + h) * 64 + s) * 20;
    float pv[DH];
#pragma unroll
    for (int i = 0; i < 4; i++) {
      u32x2 w = *(const u32x2*)(pp + 4 * i);
      pv[4 * i] = lo2f(w[0]); pv[4 * i + 1] = hi2f(w[0]); pv[4 * i + 2] = lo2f(w[1]); pv[4 * i + 3] = hi2f(w[1]);
    }
    { unsigned w = *(const unsigned*)(pp + 16); pv[16] = lo2f(w); pv[17] = hi2f(w); }
    float mx = pv[0], mn = pv[0];
#pragma unroll
    for (int f = 1; f < DH; f++) { mx = fmaxf(mx, pv[f]); mn = fminf(mn, pv[f]); }
    float e1[DH], e2[DH], s1 = 0.f, s2 = 0.f;
#pragma unroll
    for (int f = 0; f < DH; f++) {
      e1[f] = __expf(pv[f] - mx); s1 += e1[f];
      e2[f] = __expf(mn - pv[f]); s2 += e2[f];
    }
    const float r1 = 1.0f / s1, r2 = 1.0f / s2;
    if (qk == 0) {
      unsigned o[20];
#pragma unroll
      for (int f = 0; f < 9; f++) o[f] = pack2(e1[2 * f] * r1, e1[2 * f + 1] * r1);
#pragma unroll
      for (int f = 0; f < 9; f++) o[9 + f] = pack2(e2[2 * f] * r2, e2[2 * f + 1] * r2);
      o[18] = 0u; o[19] = 0u;
      u32x4* dst = (u32x4*)(phiq + ((size_t)(hg * 4 + h) * S_LEN + mt * 64 + s) * 40);
#pragma unroll
      for (int i = 0; i < 5; i++) dst[i] = (u32x4){o[4 * i], o[4 * i + 1], o[4 * i + 2], o[4 * i + 3]};
    } else {
#pragma unroll
      for (int f = 0; f < DH; f++) {
        sPhiT[(h * 48 + f) * 64 + s] = f2bf(e1[f] * r1);
        sPhiT[(h * 48 + DH + f) * 64 + s] = f2bf(e2[f] * r2);
      }
    }
  }
  __syncthreads();
  {
    const int h = wid & 3, ks = wid >> 2;
    const char* vb = (const char*)(sQKV + ((2 * 4 + h) * 64) * 32);
    f32x4 c[3][2];
    bf16x8 b[2];
    const int q = fr >> 2, p = fr & 3;
#pragma unroll
    for (int dt = 0; dt < 2; dt++) {
      const char* base = vb + (ks * 32 + 8 * fq + q) * 64 + (dt * 16 + 4 * p) * 2;
      s16x4 b0 = tr_read(base);
      s16x4 b1 = tr_read(base + 4 * 64);
      b[dt] = __builtin_shufflevector(b0, b1, 0, 1, 2, 3, 4, 5, 6, 7);
    }
#pragma unroll
    for (int ft = 0; ft < 3; ft++) {
      bf16x8 a = *(const bf16x8*)(sPhiT + (h * 48 + ft * 16 + fr) * 64 + ks * 32 + fq * 8);
#pragma unroll
      for (int dt = 0; dt < 2; dt++) c[ft][dt] = MFMA16(a, b[dt], ((f32x4){0.f, 0.f, 0.f, 0.f}), 0, 0, 0);
    }
    float* kvh = kvacc + ((size_t)(mt * 2 + ks) * NH + hg * 4 + h) * NF * 20;
#pragma unroll
    for (int ft = 0; ft < 3; ft++)
#pragma unroll
      for (int dt = 0; dt < 2; dt++) {
        const int d = dt * 16 + fr;
#pragma unroll
        for (int r = 0; r < 4; r++) {
          const int f = ft * 16 + fq * 4 + r;
          if (f < NF && d < 20) kvh[f * 20 + d] = c[ft][dt][r];
        }
      }
  }
}

#define A2_BSTAGE 18432
__global__ __launch_bounds__(512) void k_a2(const u16* __restrict__ phiq, const u16* __restrict__ kvacc, const u16* __restrict__ wo,
                                            const u16* __restrict__ xin, u16* __restrict__ xout) {
  extern __shared__ __attribute__((aligned(16))) char smem[];
  char* sO = smem;
  u16* sKV = (u16*)(smem + 36864);
  char* sB = smem + 36864 + 51200;
  const int tid = threadIdx.x, wid = tid >> 6, lane = tid & 63;
  const int bid = blockIdx.x;
  const int nt = bid & 3, mt = bid >> 2;
  const int fr = lane & 15, fq = lane >> 4;

  auto stageB = [&](int buf, int kt) {
    char* dst = sB + buf * A2_BSTAGE;
#pragma unroll
    for (int j = 0; j < 3; j++) {
      int i = wid + 8 * j;
      if (i < 18) {
        int cid = i * 64 + lane;
        int krow = cid / 18, c = cid - krow * 18;
        const u16* g = wo + (size_t)(kt * 64 + krow) * D_DIM + nt * 144 + c * 8;
        glds16(g, dst + i * 1024);
      }
    }
  };
  stageB(0, 0);
  stageB(1, 1);
  bf16x8 pa[4][2][2];
#pragma unroll
  for (int hh = 0; hh < 4; hh++)
#pragma unroll
    for (int rt = 0; rt < 2; rt++) {
      const u16* pq = phiq + ((size_t)(wid * 4 + hh) * S_LEN + mt * 32 + rt * 16 + fr) * 40;
      pa[hh][rt][0] = *(const bf16x8*)(pq + fq * 8);
      pa[hh][rt][1] = *(const bf16x8*)(pq + 32);
    }
  for (int e = tid; e < 51200 / 16; e += 512) ((u32x4*)sKV)[e] = ((const u32x4*)kvacc)[e];
  __syncthreads();
#pragma unroll
  for (int hh = 0; hh < 4; hh++) {
    const int h = wid * 4 + hh;
    bf16x8 b[2][2];
    const bf16x8 z = {0, 0, 0, 0, 0, 0, 0, 0};
#pragma unroll
    for (int dt = 0; dt < 2; dt++) {
      int d = dt * 16 + fr;
      if (d > 19) d = 19;
      b[dt][0] = *(const bf16x8*)(sKV + (h * 20 + d) * 40 + fq * 8);
      b[dt][1] = (fq == 0) ? *(const bf16x8*)(sKV + (h * 20 + d) * 40 + 32) : z;
    }
#pragma unroll
    for (int rt = 0; rt < 2; rt++) {
      bf16x8 a0 = pa[hh][rt][0];
      bf16x8 a1 = (fq == 0) ? pa[hh][rt][1] : z;
      f32x4 c0 = MFMA16(a0, b[0][0], ((f32x4){0.f, 0.f, 0.f, 0.f}), 0, 0, 0);
      c0 = MFMA16(a1, b[0][1], c0, 0, 0, 0);
      f32x4 c1 = MFMA16(a0, b[1][0], ((f32x4){0.f, 0.f, 0.f, 0.f}), 0, 0, 0);
      c1 = MFMA16(a1, b[1][1], c1, 0, 0, 0);
#pragma unroll
      for (int r = 0; r < 4; r++) {
        const float dn = __shfl(c1[r], (lane & 48) + 2);
        const float den = rbf(rbf(dn) + 1e-6f);
        const int s = rt * 16 + fq * 4 + r;
        const int col0 = h * DH + fr;
        *(u16*)(sO + s * 1152 + 16 * ((col0 >> 3) ^ (s & 7)) + (col0 & 7) * 2) = f2bf(rbf(c0[r]) / den);
        if (fr < 2) {
          const int col1 = h * DH + 16 + fr;
          *(u16*)(sO + s * 1152 + 16 * ((col1 >> 3) ^ (s & 7)) + (col1 & 7) * 2) = f2bf(rbf(c1[r]) / den);
        }
      }
    }
  }
  f32x4 acc[3];
#pragma unroll
  for (int j = 0; j < 3; j++) acc[j] = (f32x4){0.f, 0.f, 0.f, 0.f};
  const int rt = wid & 1, ct = wid >> 1;
  const int ntl = (ct == 0) ? 3 : 2;
  for (int kt = 0; kt < 9; kt++) {
    if (kt < 8) { if (wid < 2) WAITV(3); else WAITV(2); } else WAITV(0);
    BARRIER();
    if (kt + 2 < 9) stageB((kt + 2) % 3, kt + 2);
    const char* bs = sB + (kt % 3) * A2_BSTAGE;
#pragma unroll
    for (int ks = 0; ks < 2; ks++) {
      bf16x8 a = lda_frag1152(sO, rt * 16 + fr, kt * 8 + ks * 4 + fq);
#pragma unroll
      for (int j = 0; j < 3; j++) {
        if (j < ntl) {
          const int t = ct + 4 * j;
          const int q = fr >> 2, p = fr & 3;
          const char* base = bs + (ks * 32 + 8 * fq + q) * 288 + (16 * t + 4 * p) * 2;
          s16x4 b0 = tr_read(base);
          s16x4 b1 = tr_read(base + 4 * 288);
          bf16x8 b = __builtin_shufflevector(b0, b1, 0, 1, 2, 3, 4, 5, 6, 7);
          acc[j] = MFMA16(a, b, acc[j], 0, 0, 0);
        }
      }
    }
  }
#pragma unroll
  for (int j = 0; j < 3; j++) {
    if (j < ntl) {
      const int col = nt * 144 + (ct + 4 * j) * 16 + fr;
#pragma unroll
      for (int r = 0; r < 4; r++) {
        const size_t idx = (size_t)(mt * 32 + rt * 16 + fq * 4 + r) * D_DIM + col;
        xout[idx] = f2bf(bf2f(xin[idx]) + rbf(acc[j][r]));
      }
    }
  }
}

#define B_BSTAGE 18432
__global__ __launch_bounds__(512) void k_b1(const u16* __restrict__ x, const u16* __restrict__ wdre, const u16* __restrict__ wdim,
                                            u16* __restrict__ aT, u16* __restrict__ bT) {
  extern __shared__ __attribute__((aligned(16))) char smem[];
  char* sA = smem;
  char* sB = smem + 73728;
  const int tid = threadIdx.x, wid = tid >> 6, lane = tid & 63;
  const int bid = blockIdx.x;
  const int nt = bid & 7, mt = bid >> 3;
  const int wr = wid >> 1, wc = wid & 1;
  const int fr = lane & 15, fq = lane >> 4;
  const u16* Bg = (nt < 4 ? wdre : wdim) + (size_t)((nt & 3) * 144) * D_DIM;
  auto stageB = [&](int buf, int kt) {
    char* dst = sB + buf * B_BSTAGE;
#pragma unroll
    for (int j = 0; j < 3; j++) {
      int i = wid + 8 * j;
      if (i < 18) {
        int row = i * 8 + (lane >> 3);
        int c = (lane & 7) ^ (row & 7);
        glds16(Bg + (size_t)row * D_DIM + kt * 64 + c * 8, dst + i * 1024);
      }
    }
  };
  stageB(0, 0);
  stageB(1, 1);
  stageB(2, 2);
  ln64_to_lds(x + (size_t)mt * 64 * D_DIM, sA, wid, lane);
  f32x4 acc[5];
#pragma unroll
  for (int j = 0; j < 5; j++) acc[j] = (f32x4){0.f, 0.f, 0.f, 0.f};
  const int ntl = wc ? 4 : 5;
  for (int kt = 0; kt < 9; kt++) {
    if (kt < 7) { if (wid < 2) WAITV(6); else WAITV(4); }
    else if (kt == 7) { if (wid < 2) WAITV(3); else WAITV(2); }
    else WAITV(0);
    BARRIER();
    if (kt + 3 < 9) stageB((kt + 3) & 3, kt + 3);
    const char* bs = sB + (kt & 3) * B_BSTAGE;
#pragma unroll
    for (int ks = 0; ks < 2; ks++) {
      bf16x8 a = lda_frag1152(sA, wr * 16 + fr, kt * 8 + ks * 4 + fq);
#pragma unroll
      for (int j = 0; j < 5; j++) {
        if (j < ntl) {
          bf16x8 b = ld_frag128(bs, (wc * 5 + j) * 16 + fr, ks * 4 + fq);
          acc[j] = MFMA16(a, b, acc[j], 0, 0, 0);
        }
      }
    }
  }
  u16* outp = (nt < 4 ? aT : bT);
#pragma unroll
  for (int j = 0; j < 5; j++) {
    if (j < ntl) {
      const int e = (nt & 3) * 144 + (wc * 5 + j) * 16 + fr;
      const int s = mt * 64 + wr * 16 + fq * 4;
      u32x2 w = {pack2(acc[j][0], acc[j][1]), pack2(acc[j][2], acc[j][3])};
      *(u32x2*)(outp + (size_t)e * S_LEN + s) = w;
    }
  }
}

__global__ __launch_bounds__(512) void k_b2(const u16* __restrict__ wre, const u16* __restrict__ wim, const u16* __restrict__ aT,
                                            const u16* __restrict__ bT, float* __restrict__ part) {
  extern __shared__ __attribute__((aligned(16))) char smem[];
  const int tid = threadIdx.x, wid = tid >> 6, lane = tid & 63;
  const int bid = blockIdx.x;
  const int xcd = bid & 7, idx = bid >> 3;
  const int kt4 = xcd >> 1, mt = (xcd & 1) * 8 + (idx >> 2), nt = idx & 3;
  const int wr = wid >> 1, wc = wid & 1;
  const int fr = lane & 15, fq = lane >> 4;
  const u16* Ag = (kt4 < 2 ? wre : wim) + (size_t)(mt * 128) * S_LEN + (kt4 & 1) * 1024;
  const u16* Bg = (kt4 < 2 ? aT : bT) + (size_t)(nt * 144) * S_LEN + (kt4 & 1) * 1024;
  auto stage = [&](int buf, int kt) {
    char* dA = smem + buf * 34816;
    char* dB = dA + 16384;
    const int rl = lane >> 3;
    const int c = (lane & 7) ^ (rl & 7);
#pragma unroll
    for (int j = 0; j < 2; j++) {
      int i = wid + 8 * j;
      glds16(Ag + (size_t)(i * 8 + rl) * S_LEN + kt * 64 + c * 8, dA + i * 1024);
    }
#pragma unroll
    for (int j = 0; j < 3; j++) {
      int i = wid + 8 * j;
      if (i < 18) glds16(Bg + (size_t)(i * 8 + rl) * S_LEN + kt * 64 + c * 8, dB + i * 1024);
    }
  };
  stage(0, 0);
  stage(1, 1);
  stage(2, 2);
  f32x4 acc[2][5];
#pragma unroll
  for (int i = 0; i < 2; i++)
#pragma unroll
    for (int j = 0; j < 5; j++) acc[i][j] = (f32x4){0.f, 0.f, 0.f, 0.f};
  const int ntl = wc ? 4 : 5;
  for (int kt = 0; kt < 16; kt++) {
    if (kt < 14) { if (wid < 2) WAITV(10); else WAITV(8); }
    else if (kt == 14) { if (wid < 2) WAITV(5); else WAITV(4); }
    else WAITV(0);
    BARRIER();
    if (kt + 3 < 16) stage((kt + 3) & 3, kt + 3);
    const char* as = smem + (kt & 3) * 34816;
    const char* bs = as + 16384;
    bf16x8 a[2][2], b[2][5];
#pragma unroll
    for (int ks = 0; ks < 2; ks++) {
#pragma unroll
      for (int i = 0; i < 2; i++) a[ks][i] = ld_frag128(as, wr * 32 + i * 16 + fr, ks * 4 + fq);
#pragma unroll
      for (int j = 0; j < 5; j++) b[ks][j] = ld_frag128(bs, ((wc * 5 + j) % 9) * 16 + fr, ks * 4 + fq);
    }
#pragma unroll
    for (int ks = 0; ks < 2; ks++)
#pragma unroll
      for (int j = 0; j < 5; j++) {
        if (j < ntl) {
#pragma unroll
          for (int i = 0; i < 2; i++) acc[i][j] = MFMA16(a[ks][i], b[ks][j], acc[i][j], 0, 0, 0);
        }
      }
  }
  float* pp = part + (size_t)kt4 * S_LEN * D_DIM;
#pragma unroll
  for (int j = 0; j < 5; j++) {
    if (j < ntl) {
      const int col = nt * 144 + (wc * 5 + j) * 16 + fr;
#pragma unroll
      for (int i = 0; i < 2; i++)
#pragma unroll
        for (int r = 0; r < 4; r++) pp[(size_t)(mt * 128 + wr * 32 + i * 16 + fq * 4 + r) * D_DIM + col] = acc[i][j][r];
    }
  }
}
__global__ __launch_bounds__(256) void k_red(const float* __restrict__ part, const u16* __restrict__ xin, u16* __restrict__ xout) {
  const int i = (blockIdx.x * 256 + threadIdx.x) * 4;
  const size_t N = (size_t)S_LEN * D_DIM;
  f32x4 p0 = *(const f32x4*)(part + i), p1 = *(const f32x4*)(part + N + i);
  f32x4 p2 = *(const f32x4*)(part + 2 * N + i), p3 = *(const f32x4*)(part + 3 * N + i);
  u32x2 xv = *(const u32x2*)(xin + i);
  float xr[4] = {lo2f(xv[0]), hi2f(xv[0]), lo2f(xv[1]), hi2f(xv[1])};
  float o[4];
#pragma unroll
  for (int j = 0; j < 4; j++) o[j] = xr[j] + rbf(rbf(p0[j] + p1[j]) - rbf(p2[j] + p3[j]));
  *(u32x2*)(xout + i) = (u32x2){pack2(o[0], o[1]), pack2(o[2], o[3])};
}

__global__ __launch_bounds__(256) void k_kvred(const float* __restrict__ slabs, u16* __restrict__ kvT) {
  const int e = blockIdx.x * 256 + threadIdx.x;
  if (e >= NH * 40 * 20) return;
  const int h = e / 800, rem = e - h * 800, f = rem / 20, d = rem - f * 20;
  float sum = 0.f;
  if (f < NF) {
    const float* p = slabs + ((size_t)h * NF + f) * 20 + d;
#pragma unroll 16
    for (int i = 0; i < 64; i++) sum += p[(size_t)i * NH * NF * 20];
  }
  kvT[(h * 20 + d) * 40 + f] = f2bf(sum);
}

extern "C" void kernel_launch(void* const* d_in, const int* in_sizes, int n_in, void* d_out, int out_size, void* d_ws, size_t ws_size,
                              hipStream_t stream) {
  const u16* A_x = (const u16*)d_in[0];
  const u16* wqkv = (const u16*)d_in[1];
  const u16* wphi = (const u16*)d_in[2];
  const u16* wo = (const u16*)d_in[3];
  const u16* ws_re = (const u16*)d_in[5];
  const u16* ws_im = (const u16*)d_in[6];
  const u16* wd_re = (const u16*)d_in[7];
  const u16* wd_im = (const u16*)d_in[8];
  u16* out = (u16*)d_out;

  char* w = (char*)d_ws;
  float* kvacc = (float*)w; w += 64 * NH * NF * 20 * 4;
  u16* kvT = (u16*)w; w += 51200;
  u16* phiq = (u16*)w; w += (size_t)NH * S_LEN * 40 * 2;
  u16* x1 = (u16*)w; w += S_LEN * D_DIM * 2;
  u16* x2 = (u16*)w; w += S_LEN * D_DIM * 2;
  u16* aT = (u16*)w; w += S_LEN * D_DIM * 2;
  u16* bT = (u16*)w; w += S_LEN * D_DIM * 2;
  float* part = (float*)w; w += (size_t)4 * S_LEN * D_DIM * 4;

  {
    hipFuncSetAttribute((const void*)k_a1, hipFuncAttributeMaxDynamicSharedMemorySize, 73728 + 3 * A1_BSTAGE + 64);
    hipFuncSetAttribute((const void*)k_a2, hipFuncAttributeMaxDynamicSharedMemorySize, 36864 + 51200 + 3 * A2_BSTAGE + 64);
    hipFuncSetAttribute((const void*)k_b1, hipFuncAttributeMaxDynamicSharedMemorySize, 73728 + 4 * B_BSTAGE);
    hipFuncSetAttribute((const void*)k_b2, hipFuncAttributeMaxDynamicSharedMemorySize, 4 * 34816);
  }
  const u16* x = A_x;
  for (int layer = 0; layer < 2; layer++) {
    float* kva = kvacc;
    k_a1<<<256, 512, 73728 + 3 * A1_BSTAGE + 64, stream>>>(x, wqkv, wphi, phiq, kva);
    k_kvred<<<100, 256, 0, stream>>>(kva, kvT);
    k_a2<<<256, 512, 36864 + 51200 + 3 * A2_BSTAGE + 64, stream>>>(phiq, kvT, wo, x, x1);
    k_b1<<<256, 512, 73728 + 4 * B_BSTAGE, stream>>>(x1, wd_re, wd_im, aT, bT);
    k_b2<<<256, 512, 4 * 34816, stream>>>(ws_re, ws_im, aT, bT, part);
    u16* xo = layer == 0 ? x2 : out;
    k_red<<<S_LEN * D_DIM / 1024, 256, 0, stream>>>(part, x1, xo);
    x = xo;
  }
}
```
